```python
import torch

def _softmax(x, axis=-1):
    return torch.softmax(x, dim=axis)
def _gelu(x):
    return torch.nn.functional.gelu(x, approximate="tanh")
def _silu(x):
    return torch.nn.functional.silu(x)
def _sigmoid(x):
    return torch.sigmoid(x)
def _layernorm(x, axis=-1, eps=1e-5):
    m = x.mean(dim=axis, keepdim=True)
    v = x.var(dim=axis, keepdim=True, unbiased=False)
    return (x - m) / torch.sqrt(v + eps)
def _scatter_add(tgt, idx, src, axis=0):
    return tgt.clone().index_add_(axis, idx, src)
def _segment_sum(src, seg, n):
    out = torch.zeros((n,) + src.shape[1:], dtype=src.dtype)
    return out.index_add_(0, seg, src)
def _pad(x, pw):
    flat = tuple(v for pair in reversed(pw) for v in pair)
    return torch.nn.functional.pad(x, flat)

kernel_name = 'standalone__geglu_ffn__prompt'

def setup_inputs():

    _w = (lambda a: (lambda t: t.to(torch.bfloat16) if t.is_floating_point() else t)(torch.as_tensor(a)))
    B, S, D, F = 1, 2048, 640, 1728
    return {
        "x": _w((lambda *s: torch.randn(*s, dtype=torch.bfloat16) * 0.05)(B, S, D)),
        "w_gate": _w((lambda *s: torch.randn(*s, dtype=torch.bfloat16) * 0.05)(D, F)),
        "w_up": _w((lambda *s: torch.randn(*s, dtype=torch.bfloat16) * 0.05)(D, F)),
        "w_down": _w((lambda *s: torch.randn(*s, dtype=torch.bfloat16) * 0.05)(F, D)),
    }

def reference(x, w_gate, w_up, w_down):
    return (_gelu(x @ w_gate) * (x @ w_up)) @ w_down
```

```cpp
#include <hip/hip_runtime.h>
#include <hip/hip_bf16.h>
constexpr int M=2048, D=640, F=1728;
__device__ __forceinline__ float bf2f(unsigned short u){ return __uint_as_float(((unsigned)u)<<16); }
__device__ __forceinline__ float rbf(float x){ return __bfloat162float(__float2bfloat16(x)); }
__global__ void k1(const unsigned short* x, const unsigned short* wg, const unsigned short* wu, __hip_bfloat16* h){
  int f = blockIdx.x*64+threadIdx.x; int m = blockIdx.y;
  float g=0,u=0;
  for(int k=0;k<D;++k){ float xv=bf2f(x[m*D+k]); g+=xv*bf2f(wg[k*F+f]); u+=xv*bf2f(wu[k*F+f]); }
  g=rbf(g); u=rbf(u);
  float t = tanhf(0.7978845608028654f*(g+0.044715f*g*g*g));
  float ge = rbf(0.5f*g*(1.f+t));
  h[m*F+f]=__float2bfloat16(ge*u);
}
__global__ void k2(const unsigned short* h, const unsigned short* wd, __hip_bfloat16* out){
  int n = blockIdx.x*64+threadIdx.x; int m = blockIdx.y;
  float a=0;
  for(int k=0;k<F;++k){ a+=bf2f(h[m*F+k])*bf2f(wd[k*D+n]); }
  out[m*D+n]=__float2bfloat16(a);
}
extern "C" void kernel_launch(void* const* d_in, const int* in_sizes, int n_in,
                              void* d_out, int out_size, void* d_ws, size_t ws_size,
                              hipStream_t stream) {
  k1<<<dim3(F/64,M),64,0,stream>>>((const unsigned short*)d_in[0],(const unsigned short*)d_in[1],(const unsigned short*)d_in[2],(__hip_bfloat16*)d_ws);
  k2<<<dim3(D/64,M),64,0,stream>>>((const unsigned short*)d_ws,(const unsigned short*)d_in[3],(__hip_bfloat16*)d_out);
}
```

```cpp
#include <hip/hip_runtime.h>
#include <hip/hip_bf16.h>

typedef short bf16x8 __attribute__((ext_vector_type(8)));
typedef short s16x4 __attribute__((ext_vector_type(4)));
typedef float f32x4 __attribute__((ext_vector_type(4)));
typedef float f32x2 __attribute__((ext_vector_type(2)));
typedef unsigned u32x4 __attribute__((ext_vector_type(4)));
typedef unsigned u32x2 __attribute__((ext_vector_type(2)));
#define LAS __attribute__((address_space(3)))

constexpr int M_ = 2048, D_ = 640, F_ = 1728;
constexpr int NU = F_ / 16;
constexpr int NP = F_ / 32;
constexpr int NT = D_ / 16;

constexpr size_t WS_H = 0;
constexpr size_t WS_H_BYTES = (size_t)(M_ / 16) * NP * 1024;
constexpr size_t WS_WD = WS_H + WS_H_BYTES;
constexpr size_t WS_WD_BYTES = (size_t)NT * NP * 1024;

constexpr int K1_A = 128 * 128;
constexpr int K1_B = 64 * 224;
constexpr int K1_STAGE = K1_A + 2 * K1_B;
constexpr int K1_NS = 3;
constexpr int K1_KT = D_ / 64;

__device__ __forceinline__ unsigned pack_bf16(float lo, float hi) {
  unsigned r;
  asm("v_cvt_pk_bf16_f32 %0, %1, %2" : "=v"(r) : "v"(lo), "v"(hi));
  return r;
}

__device__ __forceinline__ float geglu(float g, float u) {
  const float c1 = -2.0f * 0.7978845608028654f * 1.4426950408889634f;
  const float c2 = c1 * 0.044715f;
  float t = g * __builtin_fmaf(g * g, c2, c1);
  float e = __builtin_amdgcn_exp2f(t);
  float r = __builtin_amdgcn_rcpf(1.0f + e);
  return (g * r) * u;
}

__global__ __launch_bounds__(512, 2) void geglu_k1(const unsigned short* __restrict__ x, const unsigned short* __restrict__ wg,
                                                   const unsigned short* __restrict__ wu, const unsigned short* __restrict__ wd,
                                                   char* __restrict__ ws) {
  __shared__ __attribute__((aligned(1024))) char sm[K1_NS * K1_STAGE];
  const int tid = threadIdx.x, lane = tid & 63;
  const int wave = __builtin_amdgcn_readfirstlane(tid >> 6);
  const int bx = blockIdx.x, xcd = bx & 7, j = bx >> 3;
  const int mi = (xcd >> 2) * 8 + (j >> 2);
  const int fj = (xcd & 3) * 4 + (j & 3);
  const int u0 = (7 * fj < NU - 7) ? 7 * fj : NU - 7;
  const int m0 = mi * 128;

  if (wave >= 4) {
    const int lw = wave - 4;
    auto rA = __builtin_amdgcn_make_buffer_rsrc((void*)x, 0, M_ * D_ * 2, 0x00020000);
    auto rB = __builtin_amdgcn_make_buffer_rsrc((void*)(lw < 2 ? wg : wu), 0, D_ * F_ * 2, 0x00020000);
    int vA[4], vB[7];
#pragma unroll
    for (int i = 0; i < 4; ++i) {
      int row = 8 * (lw * 4 + i) + (lane >> 3);
      int ch = (lane & 7) ^ ((row >> 1) & 7);
      vA[i] = (m0 + row) * (D_ * 2) + ch * 16;
    }
#pragma unroll
    for (int i = 0; i < 7; ++i) {
      int c = 64 * ((lw & 1) * 7 + i) + lane;
      int s = c / 14, ch = c - s * 14;
      int k = (s & 32) + 8 * ((s >> 2) & 3) + 4 * ((s >> 4) & 1) + (s & 3);
      vB[i] = k * (F_ * 2) + u0 * 32 + ch * 16;
    }
    const int ldsA = lw * 4096, ldsB = K1_A + (lw >> 1) * K1_B + (lw & 1) * 7168;
    auto issue = [&](int kt, int buf) {
      char* base = sm + buf * K1_STAGE;
#pragma unroll
      for (int i = 0; i < 4; ++i)
        __builtin_amdgcn_raw_ptr_buffer_load_lds(rA, (LAS void*)(base + ldsA + i * 1024), 16, vA[i], kt * 128, 0, 0);
#pragma unroll
      for (int i = 0; i < 7; ++i)
        __builtin_amdgcn_raw_ptr_buffer_load_lds(rB, (LAS void*)(base + ldsB + i * 1024), 16, vB[i], kt * (64 * F_ * 2), 0, 0);
    };
    issue(0, 0);
    issue(1, 1);
    issue(2, 2);
    asm volatile("s_waitcnt vmcnt(22)" ::: "memory");
    __builtin_amdgcn_s_barrier();
    int buf = 0;
    for (int s = 0; s < K1_KT; ++s) {
      if (s + 2 < K1_KT) asm volatile("s_waitcnt vmcnt(11)" ::: "memory");
      else asm volatile("s_waitcnt vmcnt(0)" ::: "memory");
      __builtin_amdgcn_s_barrier();
      if (s + 3 < K1_KT) issue(s + 3, buf);
      buf = (buf == K1_NS - 1) ? 0 : buf + 1;
    }
  } else {
    const int mi2 = wave >> 1, fi2 = wave & 1;
    const int l15 = lane & 15, g = lane >> 4;
    {
      const int gw = bx * 4 + wave;
#pragma unroll
      for (int i = 0; i < 3; ++i) {
        int fr = gw + 1024 * i;
        if (fr < NP * NT) {
          int P = fr / NT, nt = fr - P * NT;
          const unsigned short* src = wd + (size_t)(32 * P + 4 * g) * D_ + 16 * nt + l15;
          unsigned short e[8];
#pragma unroll
          for (int jj = 0; jj < 8; ++jj) e[jj] = src[((jj & 4) * 4 + (jj & 3)) * D_];
          u32x4 v;
#pragma unroll
          for (int jj = 0; jj < 4; ++jj) v[jj] = (unsigned)e[2 * jj] | ((unsigned)e[2 * jj + 1] << 16);
          *(u32x4*)(ws + WS_WD + ((size_t)(nt * NP + P) * 64 + lane) * 16) = v;
        }
      }
    }
    f32x4 acc[3][2][4];
    f32x4 acx[2][2];
#pragma unroll
    for (int a = 0; a < 3; ++a)
#pragma unroll
      for (int b = 0; b < 2; ++b)
#pragma unroll
        for (int c = 0; c < 4; ++c) acc[a][b][c] = (f32x4){0.f, 0.f, 0.f, 0.f};
#pragma unroll
    for (int b = 0; b < 2; ++b)
#pragma unroll
      for (int c = 0; c < 2; ++c) acx[b][c] = (f32x4){0.f, 0.f, 0.f, 0.f};

    const int swz = l15 >> 1;
    const int aoff0 = (64 * mi2 + l15) * 128 + ((g ^ swz) * 16);
    const int aoff1 = (64 * mi2 + l15) * 128 + (((4 + g) ^ swz) * 16);
    const int boff = K1_A + (4 * g + (l15 >> 2)) * 224 + (lane & 3) * 8 + fi2 * 96;
    const int boff6 = K1_A + (4 * g + (l15 >> 2)) * 224 + (lane & 3) * 8 + 6 * 32;

    bf16x8 xa[4], wa[4][2], xb[4], wb[4][2];
    auto rd = [&](const char* base, int kk, bf16x8(&xf)[4], bf16x8(&wf)[4][2]) {
#pragma unroll
      for (int mat = 0; mat < 2; ++mat)
#pragma unroll
        for (int u = 0; u < 4; ++u) {
          const char* p = base + (u < 3 ? boff + u * 32 : boff6) + mat * K1_B + kk * (32 * 224);
          s16x4 lo = __builtin_amdgcn_ds_read_tr16_b64_v4i16((LAS s16x4*)(p));
          s16x4 hi = __builtin_amdgcn_ds_read_tr16_b64_v4i16((LAS s16x4*)(p + 16 * 224));
          wf[u][mat] = __builtin_shufflevector(lo, hi, 0, 1, 2, 3, 4, 5, 6, 7);
        }
#pragma unroll
      for (int rb = 0; rb < 4; ++rb) xf[rb] = *(const bf16x8*)(base + (kk ? aoff1 : aoff0) + rb * 2048);
    };
    auto mm = [&](bf16x8(&xf)[4], bf16x8(&wf)[4][2]) {
#pragma unroll
      for (int u = 0; u < 3; ++u)
#pragma unroll
        for (int mat = 0; mat < 2; ++mat)
#pragma unroll
          for (int rb = 0; rb < 4; ++rb)
            acc[u][mat][rb] = __builtin_amdgcn_mfma_f32_16x16x32_bf16(wf[u][mat], xf[rb], acc[u][mat][rb], 0, 0, 0);
#pragma unroll
      for (int mat = 0; mat < 2; ++mat) {
        acx[mat][0] = __builtin_amdgcn_mfma_f32_16x16x32_bf16(wf[3][mat], fi2 ? xf[2] : xf[0], acx[mat][0], 0, 0, 0);
        acx[mat][1] = __builtin_amdgcn_mfma_f32_16x16x32_bf16(wf[3][mat], fi2 ? xf[3] : xf[1], acx[mat][1], 0, 0, 0);
      }
    };
    asm volatile("" ::: "memory");
    __builtin_amdgcn_s_barrier();
    asm volatile("" ::: "memory");
    rd(sm, 0, xa, wa);
    int buf = 0;
    for (int s = 0; s < K1_KT; ++s) {
      const char* cur = sm + buf * K1_STAGE;
      buf = (buf == K1_NS - 1) ? 0 : buf + 1;
      const char* nxt = sm + buf * K1_STAGE;
      rd(cur, 1, xb, wb);
      mm(xa, wa);
      asm volatile("s_waitcnt lgkmcnt(0)" ::: "memory");
      __builtin_amdgcn_s_barrier();
      asm volatile("" ::: "memory");
      rd(nxt, 0, xa, wa);
      mm(xb, wb);
    }
    const int skip = 7 * fj - u0;
    auto st = [&](const f32x4& gg, const f32x4& uu, int ul, int rb) {
      if (ul >= skip) {
        int U = u0 + ul, mb = mi * 8 + mi2 * 4 + rb;
        u32x2 v;
        v[0] = pack_bf16(geglu(gg[0], uu[0]), geglu(gg[1], uu[1]));
        v[1] = pack_bf16(geglu(gg[2], uu[2]), geglu(gg[3], uu[3]));
        *(u32x2*)(ws + WS_H + ((size_t)(mb * NP + (U >> 1)) * 64 + lane) * 16 + (U & 1) * 8) = v;
      }
    };
#pragma unroll
    for (int u = 0; u < 3; ++u)
#pragma unroll
      for (int rb = 0; rb < 4; ++rb) st(acc[u][0][rb], acc[u][1][rb], 3 * fi2 + u, rb);
    st(acx[0][0], acx[1][0], 6, 2 * fi2);
    st(acx[0][1], acx[1][1], 6, 2 * fi2 + 1);
  }
}

constexpr int K2_DEPTH = 5;
__global__ __launch_bounds__(256, 1) void geglu_k2(const char* __restrict__ ws, unsigned short* __restrict__ out) {
  __shared__ __attribute__((aligned(16))) char sm[4 * 20 * 1024];
  const int tid = threadIdx.x, lane = tid & 63;
  const int wave = __builtin_amdgcn_readfirstlane(tid >> 6);
  const int bx = blockIdx.x, xcd = bx & 7, j = bx >> 3;
  const int mt = (xcd >> 1) * 8 + (j >> 2);
  const int nb = (xcd & 1) * 4 + (j & 3);
  const int p0 = wave * 13 + (wave < 2 ? wave : 2);
  const int np = wave < 2 ? 14 : 13;
  const char* hp = ws + WS_H + ((size_t)(mt * 4) * NP + p0) * 1024 + lane * 16;
  const char* wp = ws + WS_WD + ((size_t)(nb * 5) * NP + p0) * 1024 + lane * 16;

  f32x4 acc[4][5];
#pragma unroll
  for (int a = 0; a < 4; ++a)
#pragma unroll
    for (int b = 0; b < 5; ++b) acc[a][b] = (f32x4){0.f, 0.f, 0.f, 0.f};
  bf16x8 hf[K2_DEPTH][4], wf[K2_DEPTH][5];
  auto ld = [&](int p, bf16x8(&h)[4], bf16x8(&w)[5]) {
    int pp = p < np ? p : np - 1;
#pragma unroll
    for (int a = 0; a < 4; ++a) h[a] = *(const bf16x8*)(hp + (size_t)(a * NP + pp) * 1024);
#pragma unroll
    for (int b = 0; b < 5; ++b) w[b] = *(const bf16x8*)(wp + (size_t)(b * NP + pp) * 1024);
  };
#pragma unroll
  for (int d = 0; d < K2_DEPTH; ++d) ld(d, hf[d], wf[d]);
#pragma unroll
  for (int p = 0; p < 14; ++p) {
    const int d = p % K2_DEPTH;
    if (p < np) {
#pragma unroll
      for (int a = 0; a < 4; ++a)
#pragma unroll
        for (int b = 0; b < 5; ++b)
          acc[a][b] = __builtin_amdgcn_mfma_f32_16x16x32_bf16(wf[d][b], hf[d][a], acc[a][b], 0, 0, 0);
    }
    __builtin_amdgcn_sched_barrier(0);
    if (p + K2_DEPTH < 14) ld(p + K2_DEPTH, hf[d], wf[d]);
    __builtin_amdgcn_sched_barrier(0);
  }
#pragma unroll
  for (int a = 0; a < 4; ++a)
#pragma unroll
    for (int b = 0; b < 5; ++b)
      *(f32x4*)(sm + ((wave * 4 + a) * 5 + b) * 1024 + lane * 16) = acc[a][b];
  __syncthreads();
  const int l15 = lane & 15, g = lane >> 4;
  unsigned short* op = out + (size_t)(mt * 64 + wave * 16 + l15) * D_ + nb * 80 + 4 * g;
#pragma unroll
  for (int b = 0; b < 5; ++b) {
    f32x4 s = *(const f32x4*)(sm + ((0 * 4 + wave) * 5 + b) * 1024 + lane * 16);
#pragma unroll
    for (int w = 1; w < 4; ++w) s += *(const f32x4*)(sm + ((w * 4 + wave) * 5 + b) * 1024 + lane * 16);
    u32x2 v;
    v[0] = pack_bf16(s[0], s[1]);
    v[1] = pack_bf16(s[2], s[3]);
    *(u32x2*)(op + b * 16) = v;
  }
}

extern "C" void kernel_launch(void* const* d_in, const int* in_sizes, int n_in, void* d_out, int out_size, void* d_ws,
                              size_t ws_size, hipStream_t stream) {
  geglu_k1<<<256, 512, 0, stream>>>((const unsigned short*)d_in[0], (const unsigned short*)d_in[1],
                                    (const unsigned short*)d_in[2], (const unsigned short*)d_in[3], (char*)d_ws);
  geglu_k2<<<256, 256, 0, stream>>>((const char*)d_ws, (unsigned short*)d_out);
}
```

```cpp
#include <hip/hip_runtime.h>
#include <hip/hip_bf16.h>

typedef short bf16x8 __attribute__((ext_vector_type(8)));
typedef short s16x4 __attribute__((ext_vector_type(4)));
typedef float f32x4 __attribute__((ext_vector_type(4)));
typedef float f32x2 __attribute__((ext_vector_type(2)));
typedef unsigned u32x4 __attribute__((ext_vector_type(4)));
typedef unsigned u32x2 __attribute__((ext_vector_type(2)));
typedef unsigned long long u64;
#define LAS __attribute__((address_space(3)))
#define GAS __attribute__((address_space(1)))

extern "C" __device__ u64 geglu_dispatch_id() __asm("llvm.amdgcn.dispatch.id");

constexpr int M_ = 2048, D_ = 640, F_ = 1728;
constexpr int NP = F_ / 32;
constexpr int NT = D_ / 16;
constexpr int NCG = F_ / 64;
constexpr int NSP = 40;

constexpr int WS_H = 0;
constexpr int WS_H_BYTES = (M_ / 16) * NP * 1024;
constexpr int WS_WD = WS_H + WS_H_BYTES;
constexpr int WS_WD_BYTES = NT * NP * 1024;
constexpr int WS_HF = WS_WD + WS_WD_BYTES;
constexpr int WS_WF = WS_HF + 16 * 64 * 8;
constexpr int WS_END = WS_WF + 64 * 8;
#define STAMP(i) do {} while (0)
#define STAMP_OUT() do {} while (0)

constexpr int K1_A = 256 * 128;
constexpr int K1_B = 64 * 128;
constexpr int K1_STAGE = K1_A + 2 * K1_B;
constexpr int K1_NS = 3;
constexpr int K1_KT = D_ / 64;
constexpr unsigned SPIN_MAX = 1u << 22;

__device__ __forceinline__ unsigned pack_bf16(float lo, float hi) {
  unsigned r;
  asm("v_cvt_pk_bf16_f32 %0, %1, %2" : "=v"(r) : "v"(lo), "v"(hi));
  return r;
}

__device__ __forceinline__ unsigned geglu2(f32x2 g, f32x2 u) {
  const float c1 = -2.0f * 0.7978845608028654f * 1.4426950408889634f;
  const float c2 = c1 * 0.044715f;
  f32x2 t = g * ((g * g) * c2 + c1);
  f32x2 e;
  e.x = __builtin_amdgcn_exp2f(t.x);
  e.y = __builtin_amdgcn_exp2f(t.y);
  e = e + 1.0f;
  f32x2 r;
  r.x = __builtin_amdgcn_rcpf(e.x);
  r.y = __builtin_amdgcn_rcpf(e.y);
  f32x2 o = (g * u) * r;
  return pack_bf16(o.x, o.y);
}

__device__ __forceinline__ void wait_flags(const char* base, int n, u64 tag, int lane) {
  const GAS u64* p = (const GAS u64*)(base) + (lane < n ? lane : 0);
  for (unsigned spins = 0; spins < SPIN_MAX; ++spins) {
    u64 v = __hip_atomic_load(p, __ATOMIC_RELAXED, __HIP_MEMORY_SCOPE_AGENT);
    if (__all(v == tag)) break;
    __builtin_amdgcn_s_sleep(2);
  }
  __builtin_amdgcn_fence(__ATOMIC_ACQUIRE, "wavefront");
  asm volatile("" ::: "memory");
}

#define ROTF ((cl * 10 / 7 + (rl * 10 + 2) / 4 + (xcd * 5) / 4) % 10)
#define HRING 3
#define STAG 1

__global__ __launch_bounds__(512, 2) void geglu_fused(const unsigned short* __restrict__ x, const unsigned short* __restrict__ wg,
                                                      const unsigned short* __restrict__ wu, const unsigned short* __restrict__ wd,
                                                      char* __restrict__ ws, unsigned short* __restrict__ out) {
  __shared__ __attribute__((aligned(1024))) char sm[K1_NS * K1_STAGE];
  const int tid = threadIdx.x, lane = tid & 63;
  const int wave = __builtin_amdgcn_readfirstlane(tid >> 6);
  const int bx = blockIdx.x, xcd = bx & 7, j = bx >> 3;
  const int cq = xcd & 3;
  const int ncg = cq < 3 ? 7 : 6;
  const bool spare = j >= 4 * ncg;
  const int l15 = lane & 15, g = lane >> 4;
  const int rl = j & 3, cl = j >> 2;
  const int rt = (xcd >> 2) * 4 + rl;
  const int cg = cq * 7 + cl;
  u64 tag;
  {
    u64 a = geglu_dispatch_id() * 0x9E3779B97F4A7C15ull + (u64)(size_t)__builtin_amdgcn_dispatch_ptr();
    unsigned lo = (unsigned)(a ^ (a >> 32)) * 0x85EBCA6Bu + (unsigned)geglu_dispatch_id();
    tag = ((u64)((unsigned)(a >> 6) ^ 0x5A3C96E1u) << 32) | (u64)lo;
  }
  auto rWS = __builtin_amdgcn_make_buffer_rsrc((void*)ws, 0, WS_END, 0x00020000);
  STAMP(0);
  if (spare) {
    const int sp = (cq < 3 ? (xcd >> 2) * 20 + cq * 4 + (j - 28) : (xcd >> 2) * 20 + 12 + (j - 24));
    const int gw = sp * 8 + wave;
    if (gw < NCG * 10) {
      const int fb = gw / 10, nbk = gw - fb * 10;
      auto rD = __builtin_amdgcn_make_buffer_rsrc((void*)wd, 0, F_ * D_ * 2, 0x00020000);
      const int r8 = lane >> 3, c8 = lane & 7;
      char* my = sm + wave * 8192;
      const int vD = (fb * 64 + r8) * (D_ * 2) + nbk * 128 + ((c8 ^ (((r8 >> 1) & 3) << 1)) * 16);
#pragma unroll
      for (int i = 0; i < 8; ++i)
        __builtin_amdgcn_raw_ptr_buffer_load_lds(rD, (LAS void*)(my + i * 1024), 16, vD, i * (8 * D_ * 2), 0, 0);
      asm volatile("s_waitcnt vmcnt(0)" ::: "memory");
      const int q = l15 >> 2, lx = (2 * g + (q >> 1)) & 3;
#pragma unroll
      for (int pp = 0; pp < 2; ++pp)
#pragma unroll
        for (int u = 0; u < 4; ++u) {
          const char* p = my + (32 * pp + 4 * g + q) * 128 + (lane & 3) * 8 + ((u ^ lx) * 32);
          s16x4 lo = __builtin_amdgcn_ds_read_tr16_b64_v4i16((LAS s16x4*)(p));
          s16x4 hi = __builtin_amdgcn_ds_read_tr16_b64_v4i16((LAS s16x4*)(p + 2048));
          bf16x8 v = __builtin_shufflevector(lo, hi, 0, 1, 2, 3, 4, 5, 6, 7);
          __builtin_amdgcn_raw_buffer_store_b128(__builtin_bit_cast(u32x4, v), rWS,
                                                 WS_WD + (((nbk * 4 + u) * NP + fb * 2 + pp) * 64 + lane) * 16, 0, 16);
        }
    }
    asm volatile("s_waitcnt vmcnt(0) lgkmcnt(0)" ::: "memory");
    __builtin_amdgcn_s_barrier();
    asm volatile("" ::: "memory");
    if (tid == 256) __hip_atomic_store((GAS u64*)(ws + WS_WF) + sp, tag, __ATOMIC_RELAXED, __HIP_MEMORY_SCOPE_AGENT);
    STAMP(3);
  }
  if (wave < 4) {
    if (!spare) {
      const int mh = wave >> 1, fh = wave & 1;
      f32x4 acc[2][2][8];
#pragma unroll
      for (int a = 0; a < 2; ++a)
#pragma unroll
        for (int b = 0; b < 2; ++b)
#pragma unroll
          for (int c = 0; c < 8; ++c) acc[a][b][c] = (f32x4){0.f, 0.f, 0.f, 0.f};
      const int swz = l15 >> 1;
      const int aoff0 = (128 * mh + l15) * 128 + ((g ^ swz) * 16);
      const int aoff1 = (128 * mh + l15) * 128 + (((4 + g) ^ swz) * 16);
      const int q = l15 >> 2, lx = (2 * g + (q >> 1)) & 3;
      const int bo0 = K1_A + (4 * g + q) * 128 + (lane & 3) * 8 + (((2 * fh) ^ lx) * 32);
      const int bo1 = K1_A + (4 * g + q) * 128 + (lane & 3) * 8 + (((2 * fh + 1) ^ lx) * 32);

      bf16x8 xa[8], wa[2][2], xb[8], wb[2][2];
      auto rd = [&](const char* base, int kk, bf16x8(&xf)[8], bf16x8(&wf)[2][2]) {
#pragma unroll
        for (int u = 0; u < 2; ++u)
#pragma unroll
          for (int mat = 0; mat < 2; ++mat) {
            const char* p = base + (u ? bo1 : bo0) + mat * K1_B + kk * 4096;
            s16x4 lo = __builtin_amdgcn_ds_read_tr16_b64_v4i16((LAS s16x4*)(p));
            s16x4 hi = __builtin_amdgcn_ds_read_tr16_b64_v4i16((LAS s16x4*)(p + 2048));
            wf[u][mat] = __builtin_shufflevector(lo, hi, 0, 1, 2, 3, 4, 5, 6, 7);
          }
#pragma unroll
        for (int rb = 0; rb < 8; ++rb) xf[rb] = *(const bf16x8*)(base + (kk ? aoff1 : aoff0) + rb * 2048);
      };
      auto mm = [&](bf16x8(&xf)[8], bf16x8(&wf)[2][2]) {
#pragma unroll
        for (int rb = 0; rb < 8; ++rb)
#pragma unroll
          for (int u = 0; u < 2; ++u)
#pragma unroll
            for (int mat = 0; mat < 2; ++mat)
              acc[u][mat][rb] = __builtin_amdgcn_mfma_f32_16x16x32_bf16(wf[u][mat], xf[rb], acc[u][mat][rb], 0, 0, 0);
      };
      auto sched = [&]() {
#pragma unroll
        for (int i = 0; i < 8; ++i) {
          __builtin_amdgcn_sched_group_barrier(0x100, 1, 0);
          __builtin_amdgcn_sched_group_barrier(0x008, 1, 0);
        }
#pragma unroll
        for (int i = 0; i < 8; ++i) {
          __builtin_amdgcn_sched_group_barrier(0x100, 1, 0);
          __builtin_amdgcn_sched_group_barrier(0x008, 2, 0);
        }
        __builtin_amdgcn_sched_group_barrier(0x008, 8, 0);
      };
      asm volatile("" ::: "memory");
      __builtin_amdgcn_s_barrier();
      asm volatile("" ::: "memory");
      STAMP(1);
      rd(sm, 0, xa, wa);
      __builtin_amdgcn_sched_barrier(0);
      __builtin_amdgcn_s_waitcnt(0xC07F);
      __builtin_amdgcn_sched_barrier(0);
      int buf = 0;
      for (int s = 0; s < K1_KT; ++s) {
        const char* cur = sm + buf * K1_STAGE;
        buf = (buf == K1_NS - 1) ? 0 : buf + 1;
        const char* nxt = sm + buf * K1_STAGE;
        rd(cur, 1, xb, wb);
        mm(xa, wa);
        sched();
        __builtin_amdgcn_sched_barrier(0);
        __builtin_amdgcn_s_waitcnt(0xC07F);
        asm volatile("" ::: "memory");
        __builtin_amdgcn_s_barrier();
        asm volatile("" ::: "memory");
        __builtin_amdgcn_sched_barrier(0);
        rd(nxt, 0, xa, wa);
        mm(xb, wb);
        sched();
        __builtin_amdgcn_sched_barrier(0);
        __builtin_amdgcn_s_waitcnt(0xC07F);
        __builtin_amdgcn_sched_barrier(0);
      }
      STAMP(2);
      const int hoff = WS_H + (((rt * 16 + mh * 8) * NP + cg * 2 + fh) * 64 + lane) * 16;
#pragma unroll
      for (int rb = 0; rb < 8; ++rb) {
        u32x4 v;
#pragma unroll
        for (int u = 0; u < 2; ++u) {
          const f32x4 gg = acc[u][0][rb], uu = acc[u][1][rb];
          v[2 * u] = geglu2((f32x2){gg[0], gg[1]}, (f32x2){uu[0], uu[1]});
          v[2 * u + 1] = geglu2((f32x2){gg[2], gg[3]}, (f32x2){uu[2], uu[3]});
        }
        __builtin_amdgcn_raw_buffer_store_b128(v, rWS, hoff + rb * NP * 1024, 0, 16);
      }
      STAMP(3);
      asm volatile("s_waitcnt vmcnt(0)" ::: "memory");
      if (lane == 0)
        __hip_atomic_store((GAS u64*)(ws + WS_HF) + (rt * 2 + mh) * 64 + ((cg * 2 + fh) & 3) * 16 + ((cg * 2 + fh) >> 2), tag, __ATOMIC_RELAXED, __HIP_MEMORY_SCOPE_AGENT);
      STAMP(4);
    }
    STAMP_OUT();
    return;
  }
  const int lw = wave - 4;
  const int mt = xcd * 4 + (j >> 3);
  const int nb = j & 7;
  const int srot = STAG * (j & 7) * 2;
  const int np = lw < 2 ? 14 : 13;
  const int hbase = WS_H + ((mt * 4) * NP + lw) * 1024;
  const int wbase = WS_WD + ((nb * 5) * NP + lw) * 1024;
  const int vo = lane * 16;
  constexpr int WL = 7;
  char* wreg = sm + lw * 36864;
  int vW[5], vH[4];
#pragma unroll
  for (int b = 0; b < 5; ++b) vW[b] = vo + wbase + b * (NP * 1024);
#pragma unroll
  for (int a = 0; a < 4; ++a) vH[a] = vo + hbase + a * (NP * 1024);
  auto dmawd = [&]() {
#pragma unroll
    for (int sl = 0; sl < WL; ++sl)
#pragma unroll
      for (int b = 0; b < 5; ++b)
        __builtin_amdgcn_raw_ptr_buffer_load_lds(rWS, (LAS void*)(wreg + sl * 5120 + b * 1024), 16, vW[b], sl * 8192, 0, 16);
  };
  if (spare) {
    wait_flags(ws + WS_WF, NSP, tag, lane);
    dmawd();
  } else {
    const int m0 = rt * 256;
    auto rA = __builtin_amdgcn_make_buffer_rsrc((void*)x, 0, M_ * D_ * 2, 0x00020000);
    auto rB = __builtin_amdgcn_make_buffer_rsrc((void*)(lw < 2 ? wg : wu), 0, D_ * F_ * 2, 0x00020000);
    const int r8 = lane >> 3, c8 = lane & 7;
    const int vA = (m0 + 64 * lw + r8) * (D_ * 2) + ((c8 ^ (r8 >> 1)) * 16);
    const int vA1 = (m0 + 64 * lw + r8) * (D_ * 2) + ((c8 ^ (r8 >> 1) ^ 4) * 16);
    int vB[4];
#pragma unroll
    for (int i = 0; i < 4; ++i) {
      int s = 32 * (lw & 1) + 8 * i + r8;
      int k = (s & 32) + 8 * ((s >> 2) & 3) + 4 * ((s >> 4) & 1) + (s & 3);
      vB[i] = k * (F_ * 2) + cg * 128 + ((c8 ^ (((s >> 1) & 3) << 1)) * 16);
    }
    const int ldsA = lw * 8192, ldsB = K1_A + lw * 4096;
    const int rot = ROTF;
    auto issue = [&](int kt, int buf) {
      int kr = kt - 1 + (rot % 9);
      kr = kr >= 9 ? kr - 9 : kr;
      kr = kt == 0 ? 0 : kr + 1;
      char* base = sm + buf * K1_STAGE;
#pragma unroll
      for (int i = 0; i < 4; ++i)
        __builtin_amdgcn_raw_ptr_buffer_load_lds(rB, (LAS void*)(base + ldsB + i * 1024), 16, vB[i], kr * (64 * F_ * 2), 0, 0);
#pragma unroll
      for (int i = 0; i < 8; ++i)
        __builtin_amdgcn_raw_ptr_buffer_load_lds(rA, (LAS void*)(base + ldsA + i * 1024), 16, (i & 1) ? vA1 : vA,
                                                 kr * 128 + i * (8 * D_ * 2), 0, 0);
    };
    issue(0, 0);
    issue(1, 1);
    asm volatile("s_waitcnt vmcnt(12)" ::: "memory");
    __builtin_amdgcn_s_barrier();
    STAMP(1);
    issue(2, 2);
    int buf = 0;
    for (int s = 0; s < 6; ++s) {
      asm volatile("s_waitcnt vmcnt(12)" ::: "memory");
      __builtin_amdgcn_s_barrier();
      issue(s + 3, buf);
      buf = (buf == K1_NS - 1) ? 0 : buf + 1;
    }
    asm volatile("s_waitcnt vmcnt(12)" ::: "memory");
    __builtin_amdgcn_s_barrier();
    wait_flags(ws + WS_WF, NSP, tag, lane);
    issue(9, 0);
    __builtin_amdgcn_s_barrier();
    asm volatile("s_waitcnt vmcnt(0)" ::: "memory");
    __builtin_amdgcn_s_barrier();
    __builtin_amdgcn_s_barrier();
    dmawd();
    STAMP(2);
  }
  {
    f32x4 acc[4][5];
#pragma unroll
    for (int a = 0; a < 4; ++a)
#pragma unroll
      for (int b = 0; b < 5; ++b) acc[a][b] = (f32x4){0.f, 0.f, 0.f, 0.f};
    constexpr int R = HRING;
    u32x4 hf[R][4], wf[R][5];
    unsigned todo = (1u << np) - 1u, ready = 0u;
    const GAS u64* fp = (const GAS u64*)(ws + WS_HF) + (mt >> 1) * 64 + lw * 16 + (lane < np ? lane : 0);
    auto pick = [&](const bool odd) -> int {
      const unsigned pool = odd ? 0xAAAAu : 0x5555u;
      if ((ready & todo & pool) == 0u) {
        for (unsigned spins = 0;; ++spins) {
          u64 v = __hip_atomic_load(fp, __ATOMIC_RELAXED, __HIP_MEMORY_SCOPE_AGENT);
          ready = (unsigned)__ballot(v == tag);
          if (spins >= SPIN_MAX) ready = ~0u;
          if (ready & todo & pool) break;
          __builtin_amdgcn_s_sleep(2);
        }
        __builtin_amdgcn_fence(__ATOMIC_ACQUIRE, "wavefront");
        asm volatile("" ::: "memory");
      }
      const unsigned cand = ready & todo & pool;
      const int i = (__builtin_ctz((cand >> srot) | (cand << (16 - srot))) + srot) & 15;
      todo &= ~(1u << i);
      return i;
    };
    auto fetch = [&](const int i, u32x4(&h)[4], u32x4(&w)[5], const bool odd) {
#pragma unroll
      for (int a = 0; a < 4; ++a) h[a] = __builtin_amdgcn_raw_buffer_load_b128(rWS, vH[a], i * 4096, 16);
      if (!odd) {
        const char* p = wreg + i * 2560 + vo;
#pragma unroll
        for (int b = 0; b < 5; ++b) w[b] = *(const u32x4*)(p + b * 1024);
      } else {
#pragma unroll
        for (int b = 0; b < 5; ++b) w[b] = __builtin_amdgcn_raw_buffer_load_b128(rWS, vW[b], i * 4096, 16);
      }
    };
    __builtin_amdgcn_sched_barrier(0);
    STAMP(4);
    __builtin_amdgcn_s_waitcnt(0x0F70);
    asm volatile("" ::: "memory");
#pragma unroll
    for (int d = 0; d < R - 1; ++d) {
      const int i = pick(d & 1);
      fetch(i, hf[d], wf[d], d & 1);
      if (d == 0) STAMP(5);
    }
#pragma unroll
    for (int k = 0; k < 14; ++k) {
      const bool more = k + R - 1 < 13;
      int i = 0;
      if (more) i = pick((k + R - 1) & 1);
      else if (k + R - 1 == 13 && np == 14) { i = pick(true); fetch(i, hf[13 % R], wf[13 % R], true); }
      __builtin_amdgcn_sched_barrier(0);
      if (k < 13 || np == 14) {
        if (more) fetch(i, hf[(k + R - 1) % R], wf[(k + R - 1) % R], (k + R - 1) & 1);
#pragma unroll
        for (int a = 0; a < 4; ++a)
#pragma unroll
          for (int b = 0; b < 5; ++b)
            acc[a][b] = __builtin_amdgcn_mfma_f32_16x16x32_bf16(__builtin_bit_cast(bf16x8, wf[k % R][b]),
                                                                __builtin_bit_cast(bf16x8, hf[k % R][a]), acc[a][b], 0, 0, 0);
        if (more) {
#pragma unroll
          for (int q = 0; q < 9; ++q) {
            __builtin_amdgcn_sched_group_barrier(0x008, 2, 0);
            __builtin_amdgcn_sched_group_barrier(0x120, 1, 0);
          }
          __builtin_amdgcn_sched_group_barrier(0x008, 2, 0);
        }
      }
      __builtin_amdgcn_sched_barrier(0);
    }
    STAMP(6);
#pragma unroll
    for (int a = 0; a < 4; ++a)
#pragma unroll
      for (int b = 0; b < 5; ++b) *(f32x4*)(wreg + (a * 5 + b) * 1024 + vo) = acc[a][b];
    asm volatile("s_waitcnt lgkmcnt(0)" ::: "memory");
    __builtin_amdgcn_s_barrier();
    asm volatile("" ::: "memory");
    unsigned short* op = out + (size_t)(mt * 64 + lw * 16 + l15) * D_ + nb * 80 + 4 * g;
#pragma unroll
    for (int b = 0; b < 5; ++b) {
      const char* p = sm + (lw * 5 + b) * 1024 + vo;
      f32x4 s = *(const f32x4*)(p);
#pragma unroll
      for (int w = 1; w < 4; ++w) s += *(const f32x4*)(p + w * 36864);
      u32x2 v;
      v[0] = pack_bf16(s[0], s[1]);
      v[1] = pack_bf16(s[2], s[3]);
      *(u32x2*)(op + b * 16) = v;
    }
  }
  STAMP_OUT();
}

extern "C" void kernel_launch(void* const* d_in, const int* in_sizes, int n_in, void* d_out, int out_size, void* d_ws,
                              size_t ws_size, hipStream_t stream) {
  geglu_fused<<<256, 512, 0, stream>>>((const unsigned short*)d_in[0], (const unsigned short*)d_in[1],
                                       (const unsigned short*)d_in[2], (const unsigned short*)d_in[3], (char*)d_ws,
                                       (unsigned short*)d_out);
}
```

```cpp
#include <hip/hip_runtime.h>
#include <hip/hip_bf16.h>

typedef short bf16x8 __attribute__((ext_vector_type(8)));
typedef short s16x4 __attribute__((ext_vector_type(4)));
typedef float f32x4 __attribute__((ext_vector_type(4)));
typedef float f32x2 __attribute__((ext_vector_type(2)));
typedef unsigned u32x4 __attribute__((ext_vector_type(4)));
typedef unsigned u32x2 __attribute__((ext_vector_type(2)));
#define LAS __attribute__((address_space(3)))

constexpr int M_ = 2048, D_ = 640, F_ = 1728;
constexpr int NP = F_ / 32;
constexpr int NT = D_ / 16;

constexpr size_t WS_H = 0;
constexpr size_t WS_H_BYTES = (size_t)(M_ / 16) * NP * 1024;
constexpr size_t WS_WD = WS_H + WS_H_BYTES;
constexpr size_t WS_WD_BYTES = (size_t)NT * NP * 1024;
#define STAMP(i) do {} while (0)

constexpr int K1_A = 256 * 128;
constexpr int K1_B = 64 * 128;
constexpr int K1_STAGE = K1_A + 2 * K1_B;
constexpr int K1_NS = 3;
constexpr int K1_KT = D_ / 64;

__device__ __forceinline__ unsigned pack_bf16(float lo, float hi) {
  unsigned r;
  asm("v_cvt_pk_bf16_f32 %0, %1, %2" : "=v"(r) : "v"(lo), "v"(hi));
  return r;
}

__device__ __forceinline__ float geglu(float g, float u) {
  const float c1 = -2.0f * 0.7978845608028654f * 1.4426950408889634f;
  const float c2 = c1 * 0.044715f;
  float t = g * __builtin_fmaf(g * g, c2, c1);
  float e = __builtin_amdgcn_exp2f(t);
  float r = __builtin_amdgcn_rcpf(1.0f + e);
  return (g * r) * u;
}

#define ST_AUX 16
#define ROTF ((cl*10/7+(rl*10+2)/4+(xcd*5)/4)%10)

__global__ __launch_bounds__(512, 2) void geglu_k1(const unsigned short* __restrict__ x, const unsigned short* __restrict__ wg,
                                                   const unsigned short* __restrict__ wu, const unsigned short* __restrict__ wd,
                                                   char* __restrict__ ws) {
  __shared__ __attribute__((aligned(1024))) char sm[K1_NS * K1_STAGE];
  const int tid = threadIdx.x, lane = tid & 63;
  const int wave = __builtin_amdgcn_readfirstlane(tid >> 6);
  const int bx = blockIdx.x, xcd = bx & 7, j = bx >> 3;
  const int cq = xcd & 3;
  const int ncg = cq < 3 ? 7 : 6;
  const int l15 = lane & 15, g = lane >> 4;
  STAMP(0);
  if (j >= 4 * ncg) {
    const int sp = (cq < 3 ? (xcd >> 2) * 20 + cq * 4 + (j - 28) : (xcd >> 2) * 20 + 12 + (j - 24));
    const int gw = sp * 8 + wave;
    for (int fr = gw; fr < NP * NT; fr += 320) {
      int P = fr / NT, nt = fr - P * NT;
      const unsigned short* src = wd + (size_t)(32 * P + 4 * g) * D_ + 16 * nt + l15;
      unsigned short e[8];
#pragma unroll
      for (int jj = 0; jj < 8; ++jj) e[jj] = src[((jj & 4) * 4 + (jj & 3)) * D_];
      u32x4 v;
#pragma unroll
      for (int jj = 0; jj < 4; ++jj) v[jj] = (unsigned)e[2 * jj] | ((unsigned)e[2 * jj + 1] << 16);
      auto rW = __builtin_amdgcn_make_buffer_rsrc((void*)(ws + WS_WD), 0, (int)WS_WD_BYTES, 0x00020000);
      __builtin_amdgcn_raw_buffer_store_b128(v, rW, ((nt * NP + P) * 64 + lane) * 16, 0, ST_AUX);
    }
  } else {
    const int rl = j & 3, cl = j >> 2;
    const int rt = (xcd >> 2) * 4 + rl;
    const int cg = cq * 7 + cl;
    const int m0 = rt * 256;
    if (wave >= 4) {
      const int lw = wave - 4;
      auto rA = __builtin_amdgcn_make_buffer_rsrc((void*)x, 0, M_ * D_ * 2, 0x00020000);
      auto rB = __builtin_amdgcn_make_buffer_rsrc((void*)(lw < 2 ? wg : wu), 0, D_ * F_ * 2, 0x00020000);
      const int r8 = lane >> 3, c8 = lane & 7;
      const int vA = (m0 + 64 * lw + r8) * (D_ * 2) + ((c8 ^ (r8 >> 1)) * 16);
      const int vA1 = (m0 + 64 * lw + r8) * (D_ * 2) + ((c8 ^ (r8 >> 1) ^ 4) * 16);
      int vB[4];
#pragma unroll
      for (int i = 0; i < 4; ++i) {
        int s = 32 * (lw & 1) + 8 * i + r8;
        int k = (s & 32) + 8 * ((s >> 2) & 3) + 4 * ((s >> 4) & 1) + (s & 3);
        vB[i] = k * (F_ * 2) + cg * 128 + ((c8 ^ (((s >> 1) & 3) << 1)) * 16);
      }
      const int ldsA = lw * 8192, ldsB = K1_A + lw * 4096;
      const int rot = ROTF;
      auto issue = [&](int kt, int buf) {
        int kr = kt - 1 + (rot % 9);
        kr = kr >= 9 ? kr - 9 : kr;
        kr = kt == 0 ? 0 : kr + 1;
        char* base = sm + buf * K1_STAGE;
#pragma unroll
        for (int i = 0; i < 4; ++i)
          __builtin_amdgcn_raw_ptr_buffer_load_lds(rB, (LAS void*)(base + ldsB + i * 1024), 16, vB[i], kr * (64 * F_ * 2), 0, 0);
#pragma unroll
        for (int i = 0; i < 8; ++i)
          __builtin_amdgcn_raw_ptr_buffer_load_lds(rA, (LAS void*)(base + ldsA + i * 1024), 16, (i & 1) ? vA1 : vA,
                                                   kr * 128 + i * (8 * D_ * 2), 0, 0);
      };
      STAMP(3);
      issue(0, 0);
      issue(1, 1);
      STAMP(4);
      asm volatile("s_waitcnt vmcnt(12)" ::: "memory");
      __builtin_amdgcn_s_barrier();
      STAMP(1);
      issue(2, 2);
      int buf = 0;
      for (int s = 0; s < K1_KT; ++s) {
        if (s + 2 < K1_KT) asm volatile("s_waitcnt vmcnt(12)" ::: "memory");
        else asm volatile("s_waitcnt vmcnt(0)" ::: "memory");
        __builtin_amdgcn_s_barrier();
        if (s + 3 < K1_KT) issue(s + 3, buf);
        buf = (buf == K1_NS - 1) ? 0 : buf + 1;
      }
      STAMP(2);
    } else {
      const int mh = wave >> 1, fh = wave & 1;
      f32x4 acc[2][2][8];
#pragma unroll
      for (int a = 0; a < 2; ++a)
#pragma unroll
        for (int b = 0; b < 2; ++b)
#pragma unroll
          for (int c = 0; c < 8; ++c) acc[a][b][c] = (f32x4){0.f, 0.f, 0.f, 0.f};
      const int swz = l15 >> 1;
      const int aoff0 = (128 * mh + l15) * 128 + ((g ^ swz) * 16);
      const int aoff1 = (128 * mh + l15) * 128 + (((4 + g) ^ swz) * 16);
      const int q = l15 >> 2, lx = (2 * g + (q >> 1)) & 3;
      const int bo0 = K1_A + (4 * g + q) * 128 + (lane & 3) * 8 + (((2 * fh) ^ lx) * 32);
      const int bo1 = K1_A + (4 * g + q) * 128 + (lane & 3) * 8 + (((2 * fh + 1) ^ lx) * 32);

      bf16x8 xa[8], wa[2][2], xb[8], wb[2][2];
      auto rd = [&](const char* base, int kk, bf16x8(&xf)[8], bf16x8(&wf)[2][2]) {
#pragma unroll
        for (int u = 0; u < 2; ++u)
#pragma unroll
          for (int mat = 0; mat < 2; ++mat) {
            const char* p = base + (u ? bo1 : bo0) + mat * K1_B + kk * 4096;
            s16x4 lo = __builtin_amdgcn_ds_read_tr16_b64_v4i16((LAS s16x4*)(p));
            s16x4 hi = __builtin_amdgcn_ds_read_tr16_b64_v4i16((LAS s16x4*)(p + 2048));
            wf[u][mat] = __builtin_shufflevector(lo, hi, 0, 1, 2, 3, 4, 5, 6, 7);
          }
#pragma unroll
        for (int rb = 0; rb < 8; ++rb) xf[rb] = *(const bf16x8*)(base + (kk ? aoff1 : aoff0) + rb * 2048);
      };
      auto mm = [&](bf16x8(&xf)[8], bf16x8(&wf)[2][2]) {
#pragma unroll
        for (int rb = 0; rb < 8; ++rb)
#pragma unroll
          for (int u = 0; u < 2; ++u)
#pragma unroll
            for (int mat = 0; mat < 2; ++mat)
              acc[u][mat][rb] = __builtin_amdgcn_mfma_f32_16x16x32_bf16(wf[u][mat], xf[rb], acc[u][mat][rb], 0, 0, 0);
      };
      auto sched = [&]() {
#pragma unroll
        for (int i = 0; i < 16; ++i) {
          __builtin_amdgcn_sched_group_barrier(0x100, 1, 0);
          __builtin_amdgcn_sched_group_barrier(0x008, 1, 0);
        }
        __builtin_amdgcn_sched_group_barrier(0x008, 16, 0);
      };
      asm volatile("" ::: "memory");
      __builtin_amdgcn_s_barrier();
      asm volatile("" ::: "memory");
      STAMP(1);
      rd(sm, 0, xa, wa);
      __builtin_amdgcn_sched_barrier(0);
      __builtin_amdgcn_s_waitcnt(0xC07F);
      __builtin_amdgcn_sched_barrier(0);
      int buf = 0;
      for (int s = 0; s < K1_KT; ++s) {
        const char* cur = sm + buf * K1_STAGE;
        buf = (buf == K1_NS - 1) ? 0 : buf + 1;
        const char* nxt = sm + buf * K1_STAGE;
        rd(cur, 1, xb, wb);
        mm(xa, wa);
        sched();
        __builtin_amdgcn_sched_barrier(0);
        __builtin_amdgcn_s_waitcnt(0xC07F);
        asm volatile("" ::: "memory");
        __builtin_amdgcn_s_barrier();
        asm volatile("" ::: "memory");
        __builtin_amdgcn_sched_barrier(0);
        rd(nxt, 0, xa, wa);
        mm(xb, wb);
        sched();
        __builtin_amdgcn_sched_barrier(0);
        __builtin_amdgcn_s_waitcnt(0xC07F);
        __builtin_amdgcn_sched_barrier(0);
      }
      STAMP(2);
      char* hp = ws + WS_H + ((size_t)((rt * 16 + mh * 8) * NP + cg * 2 + fh) * 64 + lane) * 16;
      auto rH = __builtin_amdgcn_make_buffer_rsrc((void*)(ws + WS_H), 0, (int)WS_H_BYTES, 0x00020000);
      const int hoff = (((rt * 16 + mh * 8) * NP + cg * 2 + fh) * 64 + lane) * 16;
#pragma unroll
      for (int rb = 0; rb < 8; ++rb) {
        u32x4 v;
#pragma unroll
        for (int u = 0; u < 2; ++u) {
          const f32x4 gg = acc[u][0][rb], uu = acc[u][1][rb];
          v[2 * u] = pack_bf16(geglu(gg[0], uu[0]), geglu(gg[1], uu[1]));
          v[2 * u + 1] = pack_bf16(geglu(gg[2], uu[2]), geglu(gg[3], uu[3]));
        }
        __builtin_amdgcn_raw_buffer_store_b128(v, rH, hoff + rb * NP * 1024, 0, ST_AUX);
      }
      STAMP(3);
    }
  }
}

#define K2D 3
#define K2ROT 0
constexpr int K2_DEPTH = K2D;
__global__ __launch_bounds__(256, 1) void geglu_k2(const char* __restrict__ ws, unsigned short* __restrict__ out) {
  __shared__ __attribute__((aligned(16))) char sm[4 * 20 * 1024];
  const int tid = threadIdx.x, lane = tid & 63;
  const int wave = __builtin_amdgcn_readfirstlane(tid >> 6);
  const int bx = blockIdx.x, xcd = bx & 7, j = bx >> 3;
  const int mt = (xcd >> 1) * 8 + (j >> 2);
  const int nb = (xcd & 1) * 4 + (j & 3);
  STAMP(0);
  const int p0 = wave * 13 + (wave < 2 ? wave : 2);
  const int np = wave < 2 ? 14 : 13;
  const int rot2 = K2ROT;
  const char* hp = ws + WS_H + ((size_t)(mt * 4) * NP + p0) * 1024 + lane * 16;
  const char* wp = ws + WS_WD + ((size_t)(nb * 5) * NP + p0) * 1024 + lane * 16;

  f32x4 acc[4][5];
#pragma unroll
  for (int a = 0; a < 4; ++a)
#pragma unroll
    for (int b = 0; b < 5; ++b) acc[a][b] = (f32x4){0.f, 0.f, 0.f, 0.f};
  bf16x8 hf[K2_DEPTH][4], wf[K2_DEPTH][5];
  auto ld = [&](int p, bf16x8(&h)[4], bf16x8(&w)[5]) {
    int pp = p < np ? p : np - 1;
    pp += rot2; pp = pp >= np ? pp - np : pp;
#pragma unroll
    for (int a = 0; a < 4; ++a) h[a] = *(const bf16x8*)(hp + (size_t)(a * NP + pp) * 1024);
#pragma unroll
    for (int b = 0; b < 5; ++b) w[b] = *(const bf16x8*)(wp + (size_t)(b * NP + pp) * 1024);
  };
#pragma unroll
  for (int d = 0; d < K2_DEPTH; ++d) ld(d, hf[d], wf[d]);
#pragma unroll
  for (int p = 0; p < 14; ++p) {
    const int d = p % K2_DEPTH;
    if (p < np) {
#pragma unroll
      for (int a = 0; a < 4; ++a)
#pragma unroll
        for (int b = 0; b < 5; ++b)
          acc[a][b] = __builtin_amdgcn_mfma_f32_16x16x32_bf16(wf[d][b], hf[d][a], acc[a][b], 0, 0, 0);
    }
    __builtin_amdgcn_sched_barrier(0);
    if (p == 0) STAMP(1);
    if (p + K2_DEPTH < 14) ld(p + K2_DEPTH, hf[d], wf[d]);
    __builtin_amdgcn_sched_barrier(0);
  }
  STAMP(2);
#pragma unroll
  for (int a = 0; a < 4; ++a)
#pragma unroll
    for (int b = 0; b < 5; ++b)
      *(f32x4*)(sm + ((wave * 4 + a) * 5 + b) * 1024 + lane * 16) = acc[a][b];
  __syncthreads();
  STAMP(3);
  const int l15 = lane & 15, g = lane >> 4;
  unsigned short* op = out + (size_t)(mt * 64 + wave * 16 + l15) * D_ + nb * 80 + 4 * g;
#pragma unroll
  for (int b = 0; b < 5; ++b) {
    f32x4 s = *(const f32x4*)(sm + ((0 * 4 + wave) * 5 + b) * 1024 + lane * 16);
#pragma unroll
    for (int w = 1; w < 4; ++w) s += *(const f32x4*)(sm + ((w * 4 + wave) * 5 + b) * 1024 + lane * 16);
    u32x2 v;
    v[0] = pack_bf16(s[0], s[1]);
    v[1] = pack_bf16(s[2], s[3]);
    *(u32x2*)(op + b * 16) = v;
  }
  STAMP(4);
}

extern "C" void kernel_launch(void* const* d_in, const int* in_sizes, int n_in, void* d_out, int out_size, void* d_ws,
                              size_t ws_size, hipStream_t stream) {
  geglu_k1<<<256, 512, 0, stream>>>((const unsigned short*)d_in[0], (const unsigned short*)d_in[1],
                                    (const unsigned short*)d_in[2], (const unsigned short*)d_in[3], (char*)d_ws);
  geglu_k2<<<256, 256, 0, stream>>>((const char*)d_ws, (unsigned short*)d_out);
}
```

```cpp
#include <hip/hip_runtime.h>
#include <hip/hip_bf16.h>

typedef short bf16x8 __attribute__((ext_vector_type(8)));
typedef short s16x4 __attribute__((ext_vector_type(4)));
typedef float f32x4 __attribute__((ext_vector_type(4)));
typedef float f32x2 __attribute__((ext_vector_type(2)));
typedef unsigned u32x4 __attribute__((ext_vector_type(4)));
typedef unsigned u32x2 __attribute__((ext_vector_type(2)));
typedef unsigned long long u64;
#define LAS __attribute__((address_space(3)))
#define GAS __attribute__((address_space(1)))

extern "C" __device__ u64 geglu_dispatch_id() __asm("llvm.amdgcn.dispatch.id");

constexpr int M_ = 2048, D_ = 640, F_ = 1728;
constexpr int NP = F_ / 32;
constexpr int NT = D_ / 16;
constexpr int NCG = F_ / 64;
constexpr int NSP = 40;

constexpr int WS_H = 0;
constexpr int WS_H_BYTES = (M_ / 16) * NP * 1024;
constexpr int WS_WD = WS_H + WS_H_BYTES;
constexpr int WS_WD_BYTES = NT * NP * 1024;
constexpr int WS_HF = WS_WD + WS_WD_BYTES;
constexpr int WS_WF = WS_HF + 16 * 64 * 8;
constexpr int WS_END = WS_WF + 64 * 8;
#define STAMP(i) do {} while (0)
#define STAMP_OUT() do {} while (0)

constexpr int K1_A = 256 * 128;
constexpr int K1_B = 64 * 128;
constexpr int K1_STAGE = K1_A + 2 * K1_B;
constexpr int K1_NS = 3;
constexpr int K1_KT = D_ / 64;
constexpr unsigned SPIN_MAX = 1u << 22;

__device__ __forceinline__ unsigned pack_bf16(float lo, float hi) {
  unsigned r;
  asm("v_cvt_pk_bf16_f32 %0, %1, %2" : "=v"(r) : "v"(lo), "v"(hi));
  return r;
}

__device__ __forceinline__ unsigned geglu2(f32x2 g, f32x2 u) {
  const float c1 = -2.0f * 0.7978845608028654f * 1.4426950408889634f;
  const float c2 = c1 * 0.044715f;
  f32x2 t = g * ((g * g) * c2 + c1);
  f32x2 e;
  e.x = __builtin_amdgcn_exp2f(t.x);
  e.y = __builtin_amdgcn_exp2f(t.y);
  e = e + 1.0f;
  f32x2 r;
  r.x = __builtin_amdgcn_rcpf(e.x);
  r.y = __builtin_amdgcn_rcpf(e.y);
  f32x2 o = (g * u) * r;
  return pack_bf16(o.x, o.y);
}

__device__ __forceinline__ void wait_flags(const char* base, int n, u64 tag, int lane) {
  const GAS u64* p = (const GAS u64*)(base) + (lane < n ? lane : 0);
  for (unsigned spins = 0; spins < SPIN_MAX; ++spins) {
    u64 v = __hip_atomic_load(p, __ATOMIC_RELAXED, __HIP_MEMORY_SCOPE_AGENT);
    if (__all(v == tag)) break;
    __builtin_amdgcn_s_sleep(2);
  }
  __builtin_amdgcn_fence(__ATOMIC_ACQUIRE, "wavefront");
  asm volatile("" ::: "memory");
}

#define ROTF ((cl * 10 / 7 + (rl * 10 + 2) / 4 + (xcd * 5) / 4) % 10)
#define HRING 3
#define PSLEEP 2

__global__ __launch_bounds__(512, 2) void geglu_fused(const unsigned short* __restrict__ x, const unsigned short* __restrict__ wg,
                                                      const unsigned short* __restrict__ wu, const unsigned short* __restrict__ wd,
                                                      char* __restrict__ ws, unsigned short* __restrict__ out) {
  __shared__ __attribute__((aligned(1024))) char sm[K1_NS * K1_STAGE + 64];
  const int tid = threadIdx.x, lane = tid & 63;
  const int wave = __builtin_amdgcn_readfirstlane(tid >> 6);
  const int bx = blockIdx.x, xcd = bx & 7, j = bx >> 3;
  const int cq = xcd & 3;
  const int ncg = cq < 3 ? 7 : 6;
  const bool spare = j >= 4 * ncg;
  const int l15 = lane & 15, g = lane >> 4;
  const int rl = j & 3, cl = j >> 2;
  const int rt = (xcd >> 2) * 4 + rl;
  const int cg = cq * 7 + cl;
  u64 tag;
  {
    u64 a = geglu_dispatch_id() * 0x9E3779B97F4A7C15ull + (u64)(size_t)__builtin_amdgcn_dispatch_ptr();
    unsigned lo = (unsigned)(a ^ (a >> 32)) * 0x85EBCA6Bu + (unsigned)geglu_dispatch_id();
    tag = ((u64)((unsigned)(a >> 6) ^ 0x5A3C96E1u) << 32) | (u64)lo;
  }
  auto rWS = __builtin_amdgcn_make_buffer_rsrc((void*)ws, 0, WS_END, 0x00020000);
  STAMP(0);
  const int mt = xcd * 4 + (j >> 3);
  const int nb = j & 7;
  const int lw = wave & 3;
  const int np = lw < 2 ? 14 : 13;
  LAS unsigned* rdy = (LAS unsigned*)(sm + K1_NS * K1_STAGE) + lw * 4;
  if (wave < 4 && lane == 0) *rdy = 0u;
  if (spare) {
    const int sp = (cq < 3 ? (xcd >> 2) * 20 + cq * 4 + (j - 28) : (xcd >> 2) * 20 + 12 + (j - 24));
    const int gw = sp * 8 + wave;
    if (gw < NCG * 10) {
      const int fb = gw / 10, nbk = gw - fb * 10;
      auto rD = __builtin_amdgcn_make_buffer_rsrc((void*)wd, 0, F_ * D_ * 2, 0x00020000);
      const int r8 = lane >> 3, c8 = lane & 7;
      char* my = sm + wave * 8192;
      const int vD = (fb * 64 + r8) * (D_ * 2) + nbk * 128 + ((c8 ^ (((r8 >> 1) & 3) << 1)) * 16);
#pragma unroll
      for (int i = 0; i < 8; ++i)
        __builtin_amdgcn_raw_ptr_buffer_load_lds(rD, (LAS void*)(my + i * 1024), 16, vD, i * (8 * D_ * 2), 0, 0);
      asm volatile("s_waitcnt vmcnt(0)" ::: "memory");
      const int q = l15 >> 2, lx = (2 * g + (q >> 1)) & 3;
#pragma unroll
      for (int pp = 0; pp < 2; ++pp)
#pragma unroll
        for (int u = 0; u < 4; ++u) {
          const char* p = my + (32 * pp + 4 * g + q) * 128 + (lane & 3) * 8 + ((u ^ lx) * 32);
          s16x4 lo = __builtin_amdgcn_ds_read_tr16_b64_v4i16((LAS s16x4*)(p));
          s16x4 hi = __builtin_amdgcn_ds_read_tr16_b64_v4i16((LAS s16x4*)(p + 2048));
          bf16x8 v = __builtin_shufflevector(lo, hi, 0, 1, 2, 3, 4, 5, 6, 7);
          __builtin_amdgcn_raw_buffer_store_b128(__builtin_bit_cast(u32x4, v), rWS,
                                                 WS_WD + (((nbk * 4 + u) * NP + fb * 2 + pp) * 64 + lane) * 16, 0, 16);
        }
    }
    asm volatile("s_waitcnt vmcnt(0) lgkmcnt(0)" ::: "memory");
    __builtin_amdgcn_s_barrier();
    asm volatile("" ::: "memory");
    if (tid == 256) __hip_atomic_store((GAS u64*)(ws + WS_WF) + sp, tag, __ATOMIC_RELAXED, __HIP_MEMORY_SCOPE_AGENT);
    STAMP(3);
  }
  if (wave < 4) {
    if (!spare) {
      const int mh = wave >> 1, fh = wave & 1;
      f32x4 acc[2][2][8];
#pragma unroll
      for (int a = 0; a < 2; ++a)
#pragma unroll
        for (int b = 0; b < 2; ++b)
#pragma unroll
          for (int c = 0; c < 8; ++c) acc[a][b][c] = (f32x4){0.f, 0.f, 0.f, 0.f};
      const int swz = l15 >> 1;
      const int aoff0 = (128 * mh + l15) * 128 + ((g ^ swz) * 16);
      const int aoff1 = (128 * mh + l15) * 128 + (((4 + g) ^ swz) * 16);
      const int q = l15 >> 2, lx = (2 * g + (q >> 1)) & 3;
      const int bo0 = K1_A + (4 * g + q) * 128 + (lane & 3) * 8 + (((2 * fh) ^ lx) * 32);
      const int bo1 = K1_A + (4 * g + q) * 128 + (lane & 3) * 8 + (((2 * fh + 1) ^ lx) * 32);

      bf16x8 xa[8], wa[2][2], xb[8], wb[2][2];
      auto rd = [&](const char* base, int kk, bf16x8(&xf)[8], bf16x8(&wf)[2][2]) {
#pragma unroll
        for (int u = 0; u < 2; ++u)
#pragma unroll
          for (int mat = 0; mat < 2; ++mat) {
            const char* p = base + (u ? bo1 : bo0) + mat * K1_B + kk * 4096;
            s16x4 lo = __builtin_amdgcn_ds_read_tr16_b64_v4i16((LAS s16x4*)(p));
            s16x4 hi = __builtin_amdgcn_ds_read_tr16_b64_v4i16((LAS s16x4*)(p + 2048));
            wf[u][mat] = __builtin_shufflevector(lo, hi, 0, 1, 2, 3, 4, 5, 6, 7);
          }
#pragma unroll
        for (int rb = 0; rb < 8; ++rb) xf[rb] = *(const bf16x8*)(base + (kk ? aoff1 : aoff0) + rb * 2048);
      };
      auto mm = [&](bf16x8(&xf)[8], bf16x8(&wf)[2][2]) {
#pragma unroll
        for (int rb = 0; rb < 8; ++rb)
#pragma unroll
          for (int u = 0; u < 2; ++u)
#pragma unroll
            for (int mat = 0; mat < 2; ++mat)
              acc[u][mat][rb] = __builtin_amdgcn_mfma_f32_16x16x32_bf16(wf[u][mat], xf[rb], acc[u][mat][rb], 0, 0, 0);
      };
      auto sched = [&]() {
#pragma unroll
        for (int i = 0; i < 16; ++i) {
          __builtin_amdgcn_sched_group_barrier(0x100, 1, 0);
          __builtin_amdgcn_sched_group_barrier(0x008, 1, 0);
        }
        __builtin_amdgcn_sched_group_barrier(0x008, 16, 0);
      };
      asm volatile("s_waitcnt lgkmcnt(0)" ::: "memory");
      __builtin_amdgcn_s_barrier();
      asm volatile("" ::: "memory");
      STAMP(1);
      rd(sm, 0, xa, wa);
      __builtin_amdgcn_sched_barrier(0);
      __builtin_amdgcn_s_waitcnt(0xC07F);
      __builtin_amdgcn_sched_barrier(0);
      int buf = 0;
      for (int s = 0; s < K1_KT; ++s) {
        const char* cur = sm + buf * K1_STAGE;
        buf = (buf == K1_NS - 1) ? 0 : buf + 1;
        const char* nxt = sm + buf * K1_STAGE;
        rd(cur, 1, xb, wb);
        mm(xa, wa);
        sched();
        __builtin_amdgcn_sched_barrier(0);
        __builtin_amdgcn_s_waitcnt(0xC07F);
        asm volatile("" ::: "memory");
        __builtin_amdgcn_s_barrier();
        asm volatile("" ::: "memory");
        __builtin_amdgcn_sched_barrier(0);
        rd(nxt, 0, xa, wa);
        mm(xb, wb);
        sched();
        __builtin_amdgcn_sched_barrier(0);
        __builtin_amdgcn_s_waitcnt(0xC07F);
        __builtin_amdgcn_sched_barrier(0);
      }
      STAMP(2);
      const int hoff = WS_H + (((rt * 16 + mh * 8) * NP + cg * 2 + fh) * 64 + lane) * 16;
#pragma unroll
      for (int rb = 0; rb < 8; ++rb) {
        u32x4 v;
#pragma unroll
        for (int u = 0; u < 2; ++u) {
          const f32x4 gg = acc[u][0][rb], uu = acc[u][1][rb];
          v[2 * u] = geglu2((f32x2){gg[0], gg[1]}, (f32x2){uu[0], uu[1]});
          v[2 * u + 1] = geglu2((f32x2){gg[2], gg[3]}, (f32x2){uu[2], uu[3]});
        }
        __builtin_amdgcn_raw_buffer_store_b128(v, rWS, hoff + rb * NP * 1024, 0, 16);
      }
      STAMP(3);
      asm volatile("s_waitcnt vmcnt(0)" ::: "memory");
      if (lane == 0)
        __hip_atomic_store((GAS u64*)(ws + WS_HF) + (rt * 2 + mh) * 64 + ((cg * 2 + fh) & 3) * 16 + ((cg * 2 + fh) >> 2), tag, __ATOMIC_RELAXED, __HIP_MEMORY_SCOPE_AGENT);
      STAMP(4);
    }
    {
      const GAS u64* fp = (const GAS u64*)(ws + WS_HF) + (mt >> 1) * 64 + lw * 16 + (lane < np ? lane : 0);
      const unsigned all = (1u << np) - 1u;
      unsigned seen = 0u;
      for (unsigned spins = 0; seen != all; ++spins) {
        u64 v = __hip_atomic_load(fp, __ATOMIC_RELAXED, __HIP_MEMORY_SCOPE_AGENT);
        unsigned m = (unsigned)__ballot(v == tag) & all;
        if (spins >= SPIN_MAX) m = all;
        if (m != seen) {
          seen = m;
          if (lane == 0) __hip_atomic_store(rdy, m, __ATOMIC_RELAXED, __HIP_MEMORY_SCOPE_WORKGROUP);
        } else {
          __builtin_amdgcn_s_sleep(PSLEEP);
        }
      }
    }
    STAMP_OUT();
    return;
  }
  const int vo = lane * 16;
  constexpr int WL = 7;
  char* wreg = sm + lw * 36864;
  int vW[5], vH[4];
#pragma unroll
  for (int b = 0; b < 5; ++b) vW[b] = vo + WS_WD + ((nb * 5 + b) * NP + lw) * 1024;
#pragma unroll
  for (int a = 0; a < 4; ++a) vH[a] = vo + WS_H + ((mt * 4 + a) * NP + lw) * 1024;
  auto dmawd = [&]() {
#pragma unroll
    for (int sl = 0; sl < WL; ++sl)
#pragma unroll
      for (int b = 0; b < 5; ++b)
        __builtin_amdgcn_raw_ptr_buffer_load_lds(rWS, (LAS void*)(wreg + sl * 5120 + b * 1024), 16, vW[b], sl * 8192, 0, 16);
  };
  if (spare) {
    wait_flags(ws + WS_WF, NSP, tag, lane);
    dmawd();
  } else {
    const int m0 = rt * 256;
    auto rA = __builtin_amdgcn_make_buffer_rsrc((void*)x, 0, M_ * D_ * 2, 0x00020000);
    auto rB = __builtin_amdgcn_make_buffer_rsrc((void*)(lw < 2 ? wg : wu), 0, D_ * F_ * 2, 0x00020000);
    const int r8 = lane >> 3, c8 = lane & 7;
    const int vA = (m0 + 64 * lw + r8) * (D_ * 2) + ((c8 ^ (r8 >> 1)) * 16);
    const int vA1 = (m0 + 64 * lw + r8) * (D_ * 2) + ((c8 ^ (r8 >> 1) ^ 4) * 16);
    int vB[4];
#pragma unroll
    for (int i = 0; i < 4; ++i) {
      int s = 32 * (lw & 1) + 8 * i + r8;
      int k = (s & 32) + 8 * ((s >> 2) & 3) + 4 * ((s >> 4) & 1) + (s & 3);
      vB[i] = k * (F_ * 2) + cg * 128 + ((c8 ^ (((s >> 1) & 3) << 1)) * 16);
    }
    const int ldsA = lw * 8192, ldsB = K1_A + lw * 4096;
    const int rot = ROTF;
    auto issue = [&](int kt, int buf) {
      int kr = kt - 1 + (rot % 9);
      kr = kr >= 9 ? kr - 9 : kr;
      kr = kt == 0 ? 0 : kr + 1;
      char* base = sm + buf * K1_STAGE;
#pragma unroll
      for (int i = 0; i < 4; ++i)
        __builtin_amdgcn_raw_ptr_buffer_load_lds(rB, (LAS void*)(base + ldsB + i * 1024), 16, vB[i], kr * (64 * F_ * 2), 0, 0);
#pragma unroll
      for (int i = 0; i < 8; ++i)
        __builtin_amdgcn_raw_ptr_buffer_load_lds(rA, (LAS void*)(base + ldsA + i * 1024), 16, (i & 1) ? vA1 : vA,
                                                 kr * 128 + i * (8 * D_ * 2), 0, 0);
    };
    issue(0, 0);
    issue(1, 1);
    asm volatile("s_waitcnt vmcnt(12)" ::: "memory");
    __builtin_amdgcn_s_barrier();
    STAMP(1);
    issue(2, 2);
    int buf = 0;
    for (int s = 0; s < 6; ++s) {
      asm volatile("s_waitcnt vmcnt(12)" ::: "memory");
      __builtin_amdgcn_s_barrier();
      issue(s + 3, buf);
      buf = (buf == K1_NS - 1) ? 0 : buf + 1;
    }
    asm volatile("s_waitcnt vmcnt(12)" ::: "memory");
    __builtin_amdgcn_s_barrier();
    wait_flags(ws + WS_WF, NSP, tag, lane);
    issue(9, 0);
    __builtin_amdgcn_s_barrier();
    asm volatile("s_waitcnt vmcnt(0)" ::: "memory");
    __builtin_amdgcn_s_barrier();
    __builtin_amdgcn_s_barrier();
    dmawd();
    STAMP(2);
  }
  {
    f32x4 acc[4][5];
#pragma unroll
    for (int a = 0; a < 4; ++a)
#pragma unroll
      for (int b = 0; b < 5; ++b) acc[a][b] = (f32x4){0.f, 0.f, 0.f, 0.f};
    constexpr int R = HRING;
    u32x4 hf[R][4], wo[2][5], we[2][5];
    auto ldwo = [&](const int k, u32x4(&w)[5]) {
      const int kk = (k < 13 || np == 14) ? k : 11;
#pragma unroll
      for (int b = 0; b < 5; ++b) w[b] = __builtin_amdgcn_raw_buffer_load_b128(rWS, vW[b], kk * 4096, 16);
    };
    auto ldwe = [&](const int k, u32x4(&w)[5]) {
#pragma unroll
      for (int b = 0; b < 5; ++b) w[b] = *(const u32x4*)(wreg + k * 2560 + b * 1024 + vo);
    };
    auto ldh = [&](const int k, u32x4(&h)[4]) {
#pragma unroll
      for (int a = 0; a < 4; ++a) h[a] = __builtin_amdgcn_raw_buffer_load_b128(rWS, vH[a], k * 4096, 16);
    };
    unsigned ready = 0u;
    auto ensure = [&](const int k) {
      while (!((ready >> k) & 1u)) {
        ready = __builtin_amdgcn_readfirstlane(__hip_atomic_load(rdy, __ATOMIC_RELAXED, __HIP_MEMORY_SCOPE_WORKGROUP));
        if (!((ready >> k) & 1u)) __builtin_amdgcn_s_sleep(1);
      }
      asm volatile("" ::: "memory");
    };
    ldwo(1, wo[0]);
    ldwo(3, wo[1]);
    __builtin_amdgcn_sched_barrier(0);
    STAMP(4);
    asm volatile("s_waitcnt vmcnt(10)" ::: "memory");
#pragma unroll
    for (int d = 0; d < R - 1; ++d) {
      ensure(d);
      ldh(d, hf[d]);
      if (d == 0) STAMP(5);
    }
    ldwe(0, we[0]);
#pragma unroll
    for (int k = 0; k < 14; ++k) {
      const int kn = k + R - 1;
      const bool more = kn < 13;
      if (more) ensure(kn);
      else if (kn == 13 && np == 14) { ensure(13); ldh(13, hf[13 % R]); }
      __builtin_amdgcn_sched_barrier(0);
      if (k < 13 || np == 14) {
        if (more) ldh(kn, hf[kn % R]);
        if ((k & 1) == 0 && k + 2 < 14) ldwe(k + 2, we[((k >> 1) + 1) & 1]);
#pragma unroll
        for (int a = 0; a < 4; ++a)
#pragma unroll
          for (int b = 0; b < 5; ++b)
            acc[a][b] = __builtin_amdgcn_mfma_f32_16x16x32_bf16(
                __builtin_bit_cast(bf16x8, (k & 1) ? wo[(k >> 1) & 1][b] : we[(k >> 1) & 1][b]),
                __builtin_bit_cast(bf16x8, hf[k % R][a]), acc[a][b], 0, 0, 0);
#pragma unroll
        for (int q = 0; q < 9; ++q) {
          __builtin_amdgcn_sched_group_barrier(0x008, 2, 0);
          __builtin_amdgcn_sched_group_barrier(0x120, 1, 0);
        }
        __builtin_amdgcn_sched_group_barrier(0x008, 2, 0);
      }
      __builtin_amdgcn_sched_barrier(0);
      if ((k & 1) && k + 4 < 14) ldwo(k + 4, wo[(k >> 1) & 1]);
      __builtin_amdgcn_sched_barrier(0);
    }
    STAMP(6);
#pragma unroll
    for (int a = 0; a < 4; ++a)
#pragma unroll
      for (int b = 0; b < 5; ++b) *(f32x4*)(wreg + (a * 5 + b) * 1024 + vo) = acc[a][b];
    asm volatile("s_waitcnt lgkmcnt(0)" ::: "memory");
    __builtin_amdgcn_s_barrier();
    asm volatile("" ::: "memory");
    unsigned short* op = out + (size_t)(mt * 64 + lw * 16 + l15) * D_ + nb * 80 + 4 * g;
#pragma unroll
    for (int b = 0; b < 5; ++b) {
      const char* p = sm + (lw * 5 + b) * 1024 + vo;
      f32x4 s = *(const f32x4*)(p);
#pragma unroll
      for (int w = 1; w < 4; ++w) s += *(const f32x4*)(p + w * 36864);
      u32x2 v;
      v[0] = pack_bf16(s[0], s[1]);
      v[1] = pack_bf16(s[2], s[3]);
      *(u32x2*)(op + b * 16) = v;
    }
  }
  STAMP_OUT();
}

extern "C" void kernel_launch(void* const* d_in, const int* in_sizes, int n_in, void* d_out, int out_size, void* d_ws,
                              size_t ws_size, hipStream_t stream) {
  geglu_fused<<<256, 512, 0, stream>>>((const unsigned short*)d_in[0], (const unsigned short*)d_in[1],
                                       (const unsigned short*)d_in[2], (const unsigned short*)d_in[3], (char*)d_ws,
                                       (unsigned short*)d_out);
}
```

```cpp
#include <hip/hip_runtime.h>
#include <hip/hip_bf16.h>

typedef short bf16x8 __attribute__((ext_vector_type(8)));
typedef short s16x4 __attribute__((ext_vector_type(4)));
typedef float f32x4 __attribute__((ext_vector_type(4)));
typedef float f32x2 __attribute__((ext_vector_type(2)));
typedef unsigned u32x4 __attribute__((ext_vector_type(4)));
typedef unsigned u32x2 __attribute__((ext_vector_type(2)));
typedef unsigned long long u64;
#define LAS __attribute__((address_space(3)))
#define GAS __attribute__((address_space(1)))

extern "C" __device__ u64 geglu_dispatch_id() __asm("llvm.amdgcn.dispatch.id");

constexpr int M_ = 2048, D_ = 640, F_ = 1728;
constexpr int NP = F_ / 32;
constexpr int NT = D_ / 16;
constexpr int NCG = F_ / 64;
constexpr int NSP = 40;

constexpr int WS_H = 0;
constexpr int WS_H_BYTES = (M_ / 16) * NP * 1024;
constexpr int WS_WD = WS_H + WS_H_BYTES;
constexpr int WS_WD_BYTES = NT * NP * 1024;
constexpr int WS_HF = WS_WD + WS_WD_BYTES;
constexpr int WS_WF = WS_HF + 8 * 32 * 128;
constexpr int WS_END = WS_WF + 64 * 8;

constexpr int K1_A = 256 * 128;
constexpr int K1_B = 64 * 128;
constexpr int K1_STAGE = K1_A + 2 * K1_B;
constexpr int K1_NS = 3;
constexpr int K1_KT = D_ / 64;
constexpr unsigned SPIN_MAX = 1u << 18;

__device__ __forceinline__ unsigned pack_bf16(float lo, float hi) {
  unsigned r;
  asm("v_cvt_pk_bf16_f32 %0, %1, %2" : "=v"(r) : "v"(lo), "v"(hi));
  return r;
}

__device__ __forceinline__ unsigned geglu2(f32x2 g, f32x2 u) {
  const float c1 = -2.0f * 0.7978845608028654f * 1.4426950408889634f;
  const float c2 = c1 * 0.044715f;
  f32x2 t = g * ((g * g) * c2 + c1);
  f32x2 e;
  e.x = __builtin_amdgcn_exp2f(t.x);
  e.y = __builtin_amdgcn_exp2f(t.y);
  e = e + 1.0f;
  f32x2 r;
  r.x = __builtin_amdgcn_rcpf(e.x);
  r.y = __builtin_amdgcn_rcpf(e.y);
  f32x2 o = (g * r) * u;
  return pack_bf16(o.x, o.y);
}

__device__ __forceinline__ void wait_flags(const char* base, int n, u64 tag, int lane) {
  const GAS u64* p = (const GAS u64*)(base) + (lane < n ? lane : 0);
  for (unsigned spins = 0; spins < SPIN_MAX; ++spins) {
    u64 v = __hip_atomic_load(p, __ATOMIC_RELAXED, __HIP_MEMORY_SCOPE_AGENT);
    if (__all(v == tag)) break;
    __builtin_amdgcn_s_sleep(2);
  }
  __builtin_amdgcn_fence(__ATOMIC_ACQUIRE, "wavefront");
  asm volatile("" ::: "memory");
}

#define ROTF ((cl * 10 / 7 + (rl * 10 + 2) / 4 + (xcd * 5) / 4) % 10)
constexpr int HRING = 3;
constexpr int FSTR = 16;
constexpr int PSLEEP = 2;

__global__ __launch_bounds__(512, 2) void geglu_fused(const unsigned short* __restrict__ x, const unsigned short* __restrict__ wg,
                                                      const unsigned short* __restrict__ wu, const unsigned short* __restrict__ wd,
                                                      char* __restrict__ ws, unsigned short* __restrict__ out) {
  __shared__ __attribute__((aligned(1024))) char sm[K1_NS * K1_STAGE + 64];
  const int tid = threadIdx.x, lane = tid & 63;
  const int wave = __builtin_amdgcn_readfirstlane(tid >> 6);
  const int bx = blockIdx.x, xcd = bx & 7, j = bx >> 3;
  const int cq = xcd & 3;
  const int ncg = cq < 3 ? 7 : 6;
  const bool spare = j >= 4 * ncg;
  const int l15 = lane & 15, g = lane >> 4;
  const int rl = j & 3, cl = j >> 2;
  const int rt = (xcd >> 2) * 4 + rl;
  const int cg = cq * 7 + cl;
  u64 tag;
  {
    u64 a = geglu_dispatch_id() * 0x9E3779B97F4A7C15ull + (u64)(size_t)__builtin_amdgcn_dispatch_ptr();
    unsigned lo = (unsigned)(a ^ (a >> 32)) * 0x85EBCA6Bu + (unsigned)geglu_dispatch_id();
    tag = ((u64)((unsigned)(a >> 6) ^ 0x5A3C96E1u) << 32) | (u64)lo;
  }
  auto rWS = __builtin_amdgcn_make_buffer_rsrc((void*)ws, 0, WS_END, 0x00020000);
  const int mt = xcd * 4 + (j >> 3);
  const int nb = j & 7;
  const int lw = wave & 3;
  const int np = lw < 2 ? 14 : 13;
  LAS unsigned* rdy = (LAS unsigned*)(sm + K1_NS * K1_STAGE) + lw * 4;
  LAS unsigned* cnt = (LAS unsigned*)(sm + K1_NS * K1_STAGE) + 15;
  if (wave < 4 && lane == 0) *rdy = 0u;
  if (tid == 0) *cnt = 0u;
  if (spare) {
    const int sp = (cq < 3 ? (xcd >> 2) * 20 + cq * 4 + (j - 28) : (xcd >> 2) * 20 + 12 + (j - 24));
    const int gw = sp * 8 + wave;
    if (gw < NCG * 10) {
      const int fb = gw / 10, nbk = gw - fb * 10;
      auto rD = __builtin_amdgcn_make_buffer_rsrc((void*)wd, 0, F_ * D_ * 2, 0x00020000);
      const int r8 = lane >> 3, c8 = lane & 7;
      char* my = sm + wave * 8192;
      const int vD = (fb * 64 + r8) * (D_ * 2) + nbk * 128 + ((c8 ^ (((r8 >> 1) & 3) << 1)) * 16);
#pragma unroll
      for (int i = 0; i < 8; ++i)
        __builtin_amdgcn_raw_ptr_buffer_load_lds(rD, (LAS void*)(my + i * 1024), 16, vD, i * (8 * D_ * 2), 0, 0);
      asm volatile("s_waitcnt vmcnt(0)" ::: "memory");
      const int q = l15 >> 2, lx = (2 * g + (q >> 1)) & 3;
#pragma unroll
      for (int pp = 0; pp < 2; ++pp)
#pragma unroll
        for (int u = 0; u < 4; ++u) {
          const char* p = my + (32 * pp + 4 * g + q) * 128 + (lane & 3) * 8 + ((u ^ lx) * 32);
          s16x4 lo = __builtin_amdgcn_ds_read_tr16_b64_v4i16((LAS s16x4*)(p));
          s16x4 hi = __builtin_amdgcn_ds_read_tr16_b64_v4i16((LAS s16x4*)(p + 2048));
          bf16x8 v = __builtin_shufflevector(lo, hi, 0, 1, 2, 3, 4, 5, 6, 7);
          __builtin_amdgcn_raw_buffer_store_b128(__builtin_bit_cast(u32x4, v), rWS,
                                                 WS_WD + (((nbk * 4 + u) * NP + fb * 2 + pp) * 64 + lane) * 16, 0, 16);
        }
    }
    asm volatile("s_waitcnt vmcnt(0) lgkmcnt(0)" ::: "memory");
    __builtin_amdgcn_s_barrier();
    asm volatile("" ::: "memory");
    if (tid == 256) __hip_atomic_store((GAS u64*)(ws + WS_WF) + sp, tag, __ATOMIC_RELAXED, __HIP_MEMORY_SCOPE_AGENT);
  }
  if (wave < 4) {
    if (!spare) {
      const int mh = wave >> 1, fh = wave & 1;
      f32x4 acc[2][2][8];
#pragma unroll
      for (int a = 0; a < 2; ++a)
#pragma unroll
        for (int b = 0; b < 2; ++b)
#pragma unroll
          for (int c = 0; c < 8; ++c) acc[a][b][c] = (f32x4){0.f, 0.f, 0.f, 0.f};
      const int swz = l15 >> 1;
      const int aoff0 = (128 * mh + l15) * 128 + ((g ^ swz) * 16);
      const int aoff1 = (128 * mh + l15) * 128 + (((4 + g) ^ swz) * 16);
      const int q = l15 >> 2, lx = (2 * g + (q >> 1)) & 3;
      const int bo0 = K1_A + (4 * g + q) * 128 + (lane & 3) * 8 + (((2 * fh) ^ lx) * 32);
      const int bo1 = K1_A + (4 * g + q) * 128 + (lane & 3) * 8 + (((2 * fh + 1) ^ lx) * 32);

      bf16x8 xa[8], wa[2][2], xb[8], wb[2][2];
      auto rd = [&](const char* base, int kk, bf16x8(&xf)[8], bf16x8(&wf)[2][2]) {
#pragma unroll
        for (int u = 0; u < 2; ++u)
#pragma unroll
          for (int mat = 0; mat < 2; ++mat) {
            const char* p = base + (u ? bo1 : bo0) + mat * K1_B + kk * 4096;
            s16x4 lo = __builtin_amdgcn_ds_read_tr16_b64_v4i16((LAS s16x4*)(p));
            s16x4 hi = __builtin_amdgcn_ds_read_tr16_b64_v4i16((LAS s16x4*)(p + 2048));
            wf[u][mat] = __builtin_shufflevector(lo, hi, 0, 1, 2, 3, 4, 5, 6, 7);
          }
#pragma unroll
        for (int rb = 0; rb < 8; ++rb) xf[rb] = *(const bf16x8*)(base + (kk ? aoff1 : aoff0) + rb * 2048);
      };
      auto mm = [&](bf16x8(&xf)[8], bf16x8(&wf)[2][2]) {
#pragma unroll
        for (int rb = 0; rb < 8; ++rb)
#pragma unroll
          for (int u = 0; u < 2; ++u)
#pragma unroll
            for (int mat = 0; mat < 2; ++mat)
              acc[u][mat][rb] = __builtin_amdgcn_mfma_f32_16x16x32_bf16(wf[u][mat], xf[rb], acc[u][mat][rb], 0, 0, 0);
      };
      auto sched = [&]() {
#pragma unroll
        for (int i = 0; i < 16; ++i) {
          __builtin_amdgcn_sched_group_barrier(0x100, 1, 0);
          __builtin_amdgcn_sched_group_barrier(0x008, 1, 0);
        }
        __builtin_amdgcn_sched_group_barrier(0x008, 16, 0);
      };
      asm volatile("s_waitcnt lgkmcnt(0)" ::: "memory");
      __builtin_amdgcn_s_barrier();
      asm volatile("" ::: "memory");
      rd(sm, 0, xa, wa);
      __builtin_amdgcn_sched_barrier(0);
      __builtin_amdgcn_s_waitcnt(0xC07F);
      __builtin_amdgcn_sched_barrier(0);
      int buf = 0;
      for (int s = 0; s < K1_KT; ++s) {
        const char* cur = sm + buf * K1_STAGE;
        buf = (buf == K1_NS - 1) ? 0 : buf + 1;
        const char* nxt = sm + buf * K1_STAGE;
        rd(cur, 1, xb, wb);
        mm(xa, wa);
        sched();
        __builtin_amdgcn_sched_barrier(0);
        __builtin_amdgcn_s_waitcnt(0xC07F);
        asm volatile("" ::: "memory");
        __builtin_amdgcn_s_barrier();
        asm volatile("" ::: "memory");
        __builtin_amdgcn_sched_barrier(0);
        rd(nxt, 0, xa, wa);
        mm(xb, wb);
        sched();
        __builtin_amdgcn_sched_barrier(0);
        __builtin_amdgcn_s_waitcnt(0xC07F);
        __builtin_amdgcn_sched_barrier(0);
      }
      const int hoff = WS_H + (((rt * 16 + mh * 8) * NP + cg * 2 + fh) * 64 + lane) * 16;
#pragma unroll
      for (int rb = 0; rb < 8; ++rb) {
        u32x4 v;
#pragma unroll
        for (int u = 0; u < 2; ++u) {
          const f32x4 gg = acc[u][0][rb], uu = acc[u][1][rb];
          v[2 * u] = geglu2((f32x2){gg[0], gg[1]}, (f32x2){uu[0], uu[1]});
          v[2 * u + 1] = geglu2((f32x2){gg[2], gg[3]}, (f32x2){uu[2], uu[3]});
        }
        __builtin_amdgcn_raw_buffer_store_b128(v, rWS, hoff + rb * NP * 1024, 0, 16);
      }
      asm volatile("s_waitcnt vmcnt(0)" ::: "memory");
      if (lane == 0) {
        const unsigned old = __hip_atomic_fetch_add(cnt, 1u, __ATOMIC_RELAXED, __HIP_MEMORY_SCOPE_WORKGROUP);
        if (old == 3u) __hip_atomic_store((GAS u64*)(ws + WS_HF) + rt * FSTR * 32 + cg * FSTR, tag, __ATOMIC_RELAXED, __HIP_MEMORY_SCOPE_AGENT);
      }
    }
    {
      const GAS u64* fp = (const GAS u64*)(ws + WS_HF) + (mt >> 2) * FSTR * 32 + ((4 * (lane < np ? lane : 0) + lw) >> 1) * FSTR;
      const unsigned all = (1u << np) - 1u;
      unsigned seen = 0u;
      for (unsigned spins = 0; seen != all; ++spins) {
        u64 v = __hip_atomic_load(fp, __ATOMIC_RELAXED, __HIP_MEMORY_SCOPE_AGENT);
        unsigned m = (unsigned)__ballot(v == tag) & all;
        if (spins >= SPIN_MAX) m = all;
        if (m != seen) {
          seen = m;
          if (lane == 0) __hip_atomic_store(rdy, m, __ATOMIC_RELAXED, __HIP_MEMORY_SCOPE_WORKGROUP);
        } else {
          __builtin_amdgcn_s_sleep(PSLEEP);
        }
      }
    }
    return;
  }
  const int vo = lane * 16;
  constexpr int WL = 7;
  char* wreg = sm + lw * 12288;
  auto wl = [](const int f) { return ((f / 12 + 1) % 3) * K1_STAGE + (f % 12) * 1024; };
  int vW[5], vH[4];
#pragma unroll
  for (int b = 0; b < 5; ++b) vW[b] = vo + WS_WD + ((nb * 5 + b) * NP + lw) * 1024;
#pragma unroll
  for (int a = 0; a < 4; ++a) vH[a] = vo + WS_H + ((mt * 4 + a) * NP + lw) * 1024;
  auto dmawd = [&](const int part) {
#pragma unroll
    for (int f = 12 * part; f < 12 * part + 12; ++f)
      if (f < 5 * WL)
        __builtin_amdgcn_raw_ptr_buffer_load_lds(rWS, (LAS void*)(wreg + wl(f)), 16, vW[f % 5], (f / 5) * 8192, 0, 16);
  };
  if (spare) {
    wait_flags(ws + WS_WF, NSP, tag, lane);
    dmawd(0);
    dmawd(1);
    dmawd(2);
  } else {
    const int m0 = rt * 256;
    auto rA = __builtin_amdgcn_make_buffer_rsrc((void*)x, 0, M_ * D_ * 2, 0x00020000);
    auto rB = __builtin_amdgcn_make_buffer_rsrc((void*)(lw < 2 ? wg : wu), 0, D_ * F_ * 2, 0x00020000);
    const int r8 = lane >> 3, c8 = lane & 7;
    const int vA = (m0 + 64 * lw + r8) * (D_ * 2) + ((c8 ^ (r8 >> 1)) * 16);
    const int vA1 = (m0 + 64 * lw + r8) * (D_ * 2) + ((c8 ^ (r8 >> 1) ^ 4) * 16);
    int vB[4];
#pragma unroll
    for (int i = 0; i < 4; ++i) {
      int s = 32 * (lw & 1) + 8 * i + r8;
      int k = (s & 32) + 8 * ((s >> 2) & 3) + 4 * ((s >> 4) & 1) + (s & 3);
      vB[i] = k * (F_ * 2) + cg * 128 + ((c8 ^ (((s >> 1) & 3) << 1)) * 16);
    }
    const int ldsA = lw * 8192, ldsB = K1_A + lw * 4096;
    const int rot = ROTF;
    auto issue = [&](int kt, int buf) {
      int kr = kt - 1 + (rot % 9);
      kr = kr >= 9 ? kr - 9 : kr;
      kr = kt == 0 ? 0 : kr + 1;
      char* base = sm + buf * K1_STAGE;
#pragma unroll
      for (int i = 0; i < 4; ++i)
        __builtin_amdgcn_raw_ptr_buffer_load_lds(rB, (LAS void*)(base + ldsB + i * 1024), 16, vB[i], kr * (64 * F_ * 2), 0, 0);
#pragma unroll
      for (int i = 0; i < 8; ++i)
        __builtin_amdgcn_raw_ptr_buffer_load_lds(rA, (LAS void*)(base + ldsA + i * 1024), 16, (i & 1) ? vA1 : vA,
                                                 kr * 128 + i * (8 * D_ * 2), 0, 0);
    };
    issue(0, 0);
    issue(1, 1);
    asm volatile("s_waitcnt vmcnt(12)" ::: "memory");
    __builtin_amdgcn_s_barrier();
    issue(2, 2);
    int buf = 0;
    for (int s = 0; s < 6; ++s) {
      asm volatile("s_waitcnt vmcnt(12)" ::: "memory");
      __builtin_amdgcn_s_barrier();
      issue(s + 3, buf);
      buf = (buf == K1_NS - 1) ? 0 : buf + 1;
    }
    asm volatile("s_waitcnt vmcnt(12)" ::: "memory");
    __builtin_amdgcn_s_barrier();
    wait_flags(ws + WS_WF, NSP, tag, lane);
    issue(9, 0);
    __builtin_amdgcn_s_barrier();
    dmawd(0);
    asm volatile("s_waitcnt vmcnt(12)" ::: "memory");
    __builtin_amdgcn_s_barrier();
    dmawd(1);
    __builtin_amdgcn_s_barrier();
    dmawd(2);
  }
  {
    f32x4 acc[4][5];
#pragma unroll
    for (int a = 0; a < 4; ++a)
#pragma unroll
      for (int b = 0; b < 5; ++b) acc[a][b] = (f32x4){0.f, 0.f, 0.f, 0.f};
    constexpr int R = HRING;
    u32x4 hf[R][4], wo[2][5], we[5];
    auto ldwo = [&](const int k, u32x4(&w)[5]) {
      const int kk = (k < 13 || np == 14) ? k : 11;
#pragma unroll
      for (int b = 0; b < 5; ++b) w[b] = __builtin_amdgcn_raw_buffer_load_b128(rWS, vW[b], kk * 4096, 16);
    };
    auto ldwe = [&](const int k, u32x4(&w)[5]) {
#pragma unroll
      for (int b = 0; b < 5; ++b) w[b] = *(const u32x4*)(wreg + wl(5 * (k >> 1) + b) + vo);
    };
    auto ldh = [&](const int k, u32x4(&h)[4]) {
#pragma unroll
      for (int a = 0; a < 4; ++a) h[a] = __builtin_amdgcn_raw_buffer_load_b128(rWS, vH[a], k * 4096, 16);
    };
    unsigned ready = 0u;
    auto ensure = [&](const int k) {
      while (!((ready >> k) & 1u)) {
        ready = __builtin_amdgcn_readfirstlane(__hip_atomic_load(rdy, __ATOMIC_RELAXED, __HIP_MEMORY_SCOPE_WORKGROUP));
        if (!((ready >> k) & 1u)) __builtin_amdgcn_s_sleep(1);
      }
      asm volatile("" ::: "memory");
    };
    ldwo(1, wo[0]);
    ldwo(3, wo[1]);
    __builtin_amdgcn_sched_barrier(0);
    asm volatile("s_waitcnt vmcnt(10)" ::: "memory");
#pragma unroll
    for (int d = 0; d < R - 1; ++d) {
      ensure(d);
      ldh(d, hf[d]);
    }
    ldwe(0, we);
#pragma unroll
    for (int k = 0; k < 14; ++k) {
      const int kn = k + R - 1;
      const bool more = kn < 13;
      if (more) ensure(kn);
      else if (kn == 13 && np == 14) { ensure(13); ldh(13, hf[13 % R]); }
      __builtin_amdgcn_sched_barrier(0);
      if (k < 13 || np == 14) {
        if (more) ldh(kn, hf[kn % R]);
        if ((k & 1) == 1 && k + 1 < 14) ldwe(k + 1, we);
#pragma unroll
        for (int a = 0; a < 4; ++a)
#pragma unroll
          for (int b = 0; b < 5; ++b)
            acc[a][b] = __builtin_amdgcn_mfma_f32_16x16x32_bf16(
                __builtin_bit_cast(bf16x8, (k & 1) ? wo[(k >> 1) & 1][b] : we[b]),
                __builtin_bit_cast(bf16x8, hf[k % R][a]), acc[a][b], 0, 0, 0);
#pragma unroll
        for (int q = 0; q < 9; ++q) {
          __builtin_amdgcn_sched_group_barrier(0x008, 2, 0);
          __builtin_amdgcn_sched_group_barrier(0x120, 1, 0);
        }
        __builtin_amdgcn_sched_group_barrier(0x008, 2, 0);
      }
      __builtin_amdgcn_sched_barrier(0);
      if ((k & 1) && k + 4 < 14) ldwo(k + 4, wo[(k >> 1) & 1]);
      __builtin_amdgcn_sched_barrier(0);
    }
#pragma unroll
    for (int a = 0; a < 4; ++a)
#pragma unroll
      for (int b = 0; b < 5; ++b) *(f32x4*)(wreg + K1_STAGE + (a >> 1) * K1_STAGE + ((a & 1) * 5 + b) * 1024 + vo) = acc[a][b];
    asm volatile("s_waitcnt lgkmcnt(0)" ::: "memory");
    __builtin_amdgcn_s_barrier();
    asm volatile("" ::: "memory");
    unsigned short* op = out + (size_t)(mt * 64 + lw * 16 + l15) * D_ + nb * 80 + 4 * g;
#pragma unroll
    for (int b = 0; b < 5; ++b) {
      const char* p = sm + K1_STAGE + (lw >> 1) * K1_STAGE + ((lw & 1) * 5 + b) * 1024 + vo;
      f32x4 s = *(const f32x4*)(p);
#pragma unroll
      for (int w = 1; w < 4; ++w) s += *(const f32x4*)(p + w * 12288);
      u32x2 v;
      v[0] = pack_bf16(s[0], s[1]);
      v[1] = pack_bf16(s[2], s[3]);
      *(u32x2*)(op + b * 16) = v;
    }
  }
}

extern "C" void kernel_launch(void* const* d_in, const int* in_sizes, int n_in, void* d_out, int out_size, void* d_ws,
                              size_t ws_size, hipStream_t stream) {
  geglu_fused<<<256, 512, 0, stream>>>((const unsigned short*)d_in[0], (const unsigned short*)d_in[1],
                                       (const unsigned short*)d_in[2], (const unsigned short*)d_in[3], (char*)d_ws,
                                       (unsigned short*)d_out);
}
```

```cpp
#include <hip/hip_runtime.h>
#include <hip/hip_bf16.h>

typedef short bf16x8 __attribute__((ext_vector_type(8)));
typedef short s16x4 __attribute__((ext_vector_type(4)));
typedef float f32x4 __attribute__((ext_vector_type(4)));
typedef float f32x2 __attribute__((ext_vector_type(2)));
typedef unsigned u32x4 __attribute__((ext_vector_type(4)));
typedef unsigned u32x2 __attribute__((ext_vector_type(2)));
typedef unsigned long long u64;
#define LAS __attribute__((address_space(3)))
#define GAS __attribute__((address_space(1)))

extern "C" __device__ u64 geglu_dispatch_id() __asm("llvm.amdgcn.dispatch.id");

constexpr int M_ = 2048, D_ = 640, F_ = 1728;
constexpr int NP = F_ / 32;
constexpr int NT = D_ / 16;
constexpr int NCG = F_ / 64;
constexpr int NSP = 40;

constexpr int WS_H = 0;
constexpr int WS_H_BYTES = (M_ / 16) * NP * 1024;
constexpr int WS_WD = WS_H + WS_H_BYTES;
constexpr int WS_WD_BYTES = NT * NP * 1024;
constexpr int WS_HF = WS_WD + WS_WD_BYTES;
constexpr int WS_WF = WS_HF + 8 * 32 * 128;
constexpr int WS_END = WS_WF + 64 * 8;

constexpr int K1_A = 256 * 128;
constexpr int K1_B = 64 * 128;
constexpr int K1_STAGE = K1_A + 2 * K1_B;
constexpr int K1_NS = 3;
constexpr int K1_KT = D_ / 64;
constexpr unsigned SPIN_MAX = 1u << 18;

__device__ __forceinline__ unsigned pack_bf16(float lo, float hi) {
  unsigned r;
  asm("v_cvt_pk_bf16_f32 %0, %1, %2" : "=v"(r) : "v"(lo), "v"(hi));
  return r;
}

__device__ __forceinline__ unsigned geglu2(f32x2 g, f32x2 u) {
  const float c1 = -2.0f * 0.7978845608028654f * 1.4426950408889634f;
  const float c2 = c1 * 0.044715f;
  f32x2 t = g * ((g * g) * c2 + c1);
  f32x2 e;
  e.x = __builtin_amdgcn_exp2f(t.x);
  e.y = __builtin_amdgcn_exp2f(t.y);
  e = e + 1.0f;
  f32x2 r;
  r.x = __builtin_amdgcn_rcpf(e.x);
  r.y = __builtin_amdgcn_rcpf(e.y);
  f32x2 o = (g * r) * u;
  return pack_bf16(o.x, o.y);
}

__device__ __forceinline__ void wait_flags(const char* base, int n, u64 tag, int lane) {
  const GAS u64* p = (const GAS u64*)(base) + (lane < n ? lane : 0);
  for (unsigned spins = 0; spins < SPIN_MAX; ++spins) {
    u64 v = __hip_atomic_load(p, __ATOMIC_RELAXED, __HIP_MEMORY_SCOPE_AGENT);
    if (__all(v == tag)) break;
    __builtin_amdgcn_s_sleep(2);
  }
  __builtin_amdgcn_fence(__ATOMIC_ACQUIRE, "wavefront");
  asm volatile("" ::: "memory");
}

#define ROTF ((cl * 10 / 7 + (rl * 10 + 2) / 4 + (xcd * 5) / 4) % 10)
constexpr int HRING = 3;
constexpr int FSTR = 16;
constexpr int PSLEEP = 2;

__global__ __launch_bounds__(512, 2) void geglu_fused(const unsigned short* __restrict__ x, const unsigned short* __restrict__ wg,
                                                      const unsigned short* __restrict__ wu, const unsigned short* __restrict__ wd,
                                                      char* __restrict__ ws, unsigned short* __restrict__ out) {
  __shared__ __attribute__((aligned(1024))) char sm[K1_NS * K1_STAGE + 64];
  const int tid = threadIdx.x, lane = tid & 63;
  const int wave = __builtin_amdgcn_readfirstlane(tid >> 6);
  const int bx = blockIdx.x, xcd = bx & 7, j = bx >> 3;
  const int cq = xcd & 3;
  const int ncg = cq < 3 ? 7 : 6;
  const bool spare = j >= 4 * ncg;
  const int l15 = lane & 15, g = lane >> 4;
  const int rl = j & 3, cl = j >> 2;
  const int rt = (xcd >> 2) * 4 + rl;
  const int cg = cq * 7 + cl;
  asm volatile("" ::"s"(x), "s"(wg), "s"(wu), "s"(wd), "s"(ws), "s"(out));
  const int lw = wave & 3;
  const int m0 = rt * 256;
  auto rA = __builtin_amdgcn_make_buffer_rsrc((void*)x, 0, M_ * D_ * 2, 0x00020000);
  auto rB = __builtin_amdgcn_make_buffer_rsrc((void*)(lw < 2 ? wg : wu), 0, D_ * F_ * 2, 0x00020000);
  const int r8 = lane >> 3, c8 = lane & 7;
  const int vA = (m0 + 64 * lw + r8) * (D_ * 2) + ((c8 ^ (r8 >> 1)) * 16);
  const int vA1 = (m0 + 64 * lw + r8) * (D_ * 2) + ((c8 ^ (r8 >> 1) ^ 4) * 16);
  int vB[4];
#pragma unroll
  for (int i = 0; i < 4; ++i) {
    int s = 32 * (lw & 1) + 8 * i + r8;
    int k = (s & 32) + 8 * ((s >> 2) & 3) + 4 * ((s >> 4) & 1) + (s & 3);
    vB[i] = k * (F_ * 2) + cg * 128 + ((c8 ^ (((s >> 1) & 3) << 1)) * 16);
  }
  const int ldsA = lw * 8192, ldsB = K1_A + lw * 4096;
  const int rot = ROTF;
  auto issue = [&](int kt, int buf) {
    int kr = kt + rot;
    kr = kr >= K1_KT ? kr - K1_KT : kr;
    char* base = sm + buf * K1_STAGE;
#pragma unroll
    for (int i = 0; i < 4; ++i)
      __builtin_amdgcn_raw_ptr_buffer_load_lds(rB, (LAS void*)(base + ldsB + i * 1024), 16, vB[i], kr * (64 * F_ * 2), 0, 0);
#pragma unroll
    for (int i = 0; i < 8; ++i)
      __builtin_amdgcn_raw_ptr_buffer_load_lds(rA, (LAS void*)(base + ldsA + i * 1024), 16, (i & 1) ? vA1 : vA,
                                               kr * 128 + i * (8 * D_ * 2), 0, 0);
  };
  if (wave >= 4 && !spare) {
    issue(0, 0);
    issue(1, 1);
  }
  asm volatile("" ::: "memory");
  u64 tag;
  {
    u64 a = geglu_dispatch_id() * 0x9E3779B97F4A7C15ull + (u64)(size_t)__builtin_amdgcn_dispatch_ptr();
    unsigned lo = (unsigned)(a ^ (a >> 32)) * 0x85EBCA6Bu + (unsigned)geglu_dispatch_id();
    tag = ((u64)((unsigned)(a >> 6) ^ 0x5A3C96E1u) << 32) | (u64)lo;
  }
  auto rWS = __builtin_amdgcn_make_buffer_rsrc((void*)ws, 0, WS_END, 0x00020000);
  const int mt = xcd * 4 + (j >> 3);
  const int nb = j & 7;
  const int np = lw < 2 ? 14 : 13;
  LAS unsigned* rdy = (LAS unsigned*)(sm + K1_NS * K1_STAGE) + lw * 4;
  LAS unsigned* cnt = (LAS unsigned*)(sm + K1_NS * K1_STAGE) + 15;
  if (wave < 4 && lane == 0) *rdy = 0u;
  if (tid == 0) *cnt = 0u;
  if (spare) {
    const int sp = (cq < 3 ? (xcd >> 2) * 20 + cq * 4 + (j - 28) : (xcd >> 2) * 20 + 12 + (j - 24));
    const int gw = sp * 8 + wave;
    if (gw < NCG * 10) {
      const int fb = gw / 10, nbk = gw - fb * 10;
      auto rD = __builtin_amdgcn_make_buffer_rsrc((void*)wd, 0, F_ * D_ * 2, 0x00020000);
      const int r8 = lane >> 3, c8 = lane & 7;
      char* my = sm + wave * 8192;
      const int vD = (fb * 64 + r8) * (D_ * 2) + nbk * 128 + ((c8 ^ (((r8 >> 1) & 3) << 1)) * 16);
#pragma unroll
      for (int i = 0; i < 8; ++i)
        __builtin_amdgcn_raw_ptr_buffer_load_lds(rD, (LAS void*)(my + i * 1024), 16, vD, i * (8 * D_ * 2), 0, 0);
      asm volatile("s_waitcnt vmcnt(0)" ::: "memory");
      const int q = l15 >> 2, lx = (2 * g + (q >> 1)) & 3;
#pragma unroll
      for (int pp = 0; pp < 2; ++pp)
#pragma unroll
        for (int u = 0; u < 4; ++u) {
          const char* p = my + (32 * pp + 4 * g + q) * 128 + (lane & 3) * 8 + ((u ^ lx) * 32);
          s16x4 lo = __builtin_amdgcn_ds_read_tr16_b64_v4i16((LAS s16x4*)(p));
          s16x4 hi = __builtin_amdgcn_ds_read_tr16_b64_v4i16((LAS s16x4*)(p + 2048));
          bf16x8 v = __builtin_shufflevector(lo, hi, 0, 1, 2, 3, 4, 5, 6, 7);
          __builtin_amdgcn_raw_buffer_store_b128(__builtin_bit_cast(u32x4, v), rWS,
                                                 WS_WD + (((nbk * 4 + u) * NP + fb * 2 + pp) * 64 + lane) * 16, 0, 16);
        }
    }
    asm volatile("s_waitcnt vmcnt(0) lgkmcnt(0)" ::: "memory");
    __builtin_amdgcn_s_barrier();
    asm volatile("" ::: "memory");
    if (tid == 256) __hip_atomic_store((GAS u64*)(ws + WS_WF) + sp, tag, __ATOMIC_RELAXED, __HIP_MEMORY_SCOPE_AGENT);
  }
  if (wave < 4) {
    if (!spare) {
      const int mh = wave >> 1, fh = wave & 1;
      f32x4 acc[2][2][8];
#pragma unroll
      for (int a = 0; a < 2; ++a)
#pragma unroll
        for (int b = 0; b < 2; ++b)
#pragma unroll
          for (int c = 0; c < 8; ++c) acc[a][b][c] = (f32x4){0.f, 0.f, 0.f, 0.f};
      const int swz = l15 >> 1;
      const int aoff0 = (128 * mh + l15) * 128 + ((g ^ swz) * 16);
      const int aoff1 = (128 * mh + l15) * 128 + (((4 + g) ^ swz) * 16);
      const int q = l15 >> 2, lx = (2 * g + (q >> 1)) & 3;
      const int bo0 = K1_A + (4 * g + q) * 128 + (lane & 3) * 8 + (((2 * fh) ^ lx) * 32);
      const int bo1 = K1_A + (4 * g + q) * 128 + (lane & 3) * 8 + (((2 * fh + 1) ^ lx) * 32);

      bf16x8 xa[8], wa[2][2], xb[8], wb[2][2];
      auto rd = [&](const char* base, int kk, bf16x8(&xf)[8], bf16x8(&wf)[2][2]) {
#pragma unroll
        for (int u = 0; u < 2; ++u)
#pragma unroll
          for (int mat = 0; mat < 2; ++mat) {
            const char* p = base + (u ? bo1 : bo0) + mat * K1_B + kk * 4096;
            s16x4 lo = __builtin_amdgcn_ds_read_tr16_b64_v4i16((LAS s16x4*)(p));
            s16x4 hi = __builtin_amdgcn_ds_read_tr16_b64_v4i16((LAS s16x4*)(p + 2048));
            wf[u][mat] = __builtin_shufflevector(lo, hi, 0, 1, 2, 3, 4, 5, 6, 7);
          }
#pragma unroll
        for (int rb = 0; rb < 8; ++rb) xf[rb] = *(const bf16x8*)(base + (kk ? aoff1 : aoff0) + rb * 2048);
      };
      auto mm = [&](bf16x8(&xf)[8], bf16x8(&wf)[2][2]) {
#pragma unroll
        for (int rb = 0; rb < 8; ++rb)
#pragma unroll
          for (int u = 0; u < 2; ++u)
#pragma unroll
            for (int mat = 0; mat < 2; ++mat)
              acc[u][mat][rb] = __builtin_amdgcn_mfma_f32_16x16x32_bf16(wf[u][mat], xf[rb], acc[u][mat][rb], 0, 0, 0);
      };
      auto sched = [&]() {
#pragma unroll
        for (int i = 0; i < 16; ++i) {
          __builtin_amdgcn_sched_group_barrier(0x100, 1, 0);
          __builtin_amdgcn_sched_group_barrier(0x008, 1, 0);
        }
        __builtin_amdgcn_sched_group_barrier(0x008, 16, 0);
      };
      asm volatile("s_waitcnt lgkmcnt(0)" ::: "memory");
      __builtin_amdgcn_s_barrier();
      asm volatile("" ::: "memory");
      rd(sm, 0, xa, wa);
      __builtin_amdgcn_sched_barrier(0);
      __builtin_amdgcn_s_waitcnt(0xC07F);
      __builtin_amdgcn_sched_barrier(0);
      int buf = 0;
      for (int s = 0; s < K1_KT; ++s) {
        const char* cur = sm + buf * K1_STAGE;
        buf = (buf == K1_NS - 1) ? 0 : buf + 1;
        const char* nxt = sm + buf * K1_STAGE;
        rd(cur, 1, xb, wb);
        mm(xa, wa);
        sched();
        __builtin_amdgcn_sched_barrier(0);
        __builtin_amdgcn_s_waitcnt(0xC07F);
        asm volatile("" ::: "memory");
        __builtin_amdgcn_s_barrier();
        asm volatile("" ::: "memory");
        __builtin_amdgcn_sched_barrier(0);
        rd(nxt, 0, xa, wa);
        mm(xb, wb);
        sched();
        __builtin_amdgcn_sched_barrier(0);
        __builtin_amdgcn_s_waitcnt(0xC07F);
        __builtin_amdgcn_sched_barrier(0);
      }
      const int hoff = WS_H + (((rt * 16 + mh * 8) * NP + cg * 2 + fh) * 64 + lane) * 16;
#pragma unroll
      for (int rb = 0; rb < 8; ++rb) {
        u32x4 v;
#pragma unroll
        for (int u = 0; u < 2; ++u) {
          const f32x4 gg = acc[u][0][rb], uu = acc[u][1][rb];
          v[2 * u] = geglu2((f32x2){gg[0], gg[1]}, (f32x2){uu[0], uu[1]});
          v[2 * u + 1] = geglu2((f32x2){gg[2], gg[3]}, (f32x2){uu[2], uu[3]});
        }
        __builtin_amdgcn_raw_buffer_store_b128(v, rWS, hoff + rb * NP * 1024, 0, 16);
      }
      asm volatile("s_waitcnt vmcnt(0)" ::: "memory");
      if (lane == 0) {
        const unsigned old = __hip_atomic_fetch_add(cnt, 1u, __ATOMIC_RELAXED, __HIP_MEMORY_SCOPE_WORKGROUP);
        if (old == 3u) __hip_atomic_store((GAS u64*)(ws + WS_HF) + rt * FSTR * 32 + cg * FSTR, tag, __ATOMIC_RELAXED, __HIP_MEMORY_SCOPE_AGENT);
      }
    }
    {
      const GAS u64* fp = (const GAS u64*)(ws + WS_HF) + (mt >> 2) * FSTR * 32 + ((4 * (lane < np ? lane : 0) + lw) >> 1) * FSTR;
      const unsigned all = (1u << np) - 1u;
      unsigned seen = 0u;
      for (unsigned spins = 0; seen != all; ++spins) {
        u64 v = __hip_atomic_load(fp, __ATOMIC_RELAXED, __HIP_MEMORY_SCOPE_AGENT);
        unsigned m = (unsigned)__ballot(v == tag) & all;
        if (spins >= SPIN_MAX) m = all;
        if (m != seen) {
          seen = m;
          if (lane == 0) __hip_atomic_store(rdy, m, __ATOMIC_RELAXED, __HIP_MEMORY_SCOPE_WORKGROUP);
        } else {
          __builtin_amdgcn_s_sleep(PSLEEP);
        }
      }
    }
    return;
  }
  const int vo = lane * 16;
  constexpr int WL = 7;
  char* wreg = sm + lw * 12288;
  auto wl = [](const int f) { return ((f / 12 + 1) % 3) * K1_STAGE + (f % 12) * 1024; };
  int vW[5], vH[4];
#pragma unroll
  for (int b = 0; b < 5; ++b) vW[b] = vo + WS_WD + ((nb * 5 + b) * NP + lw) * 1024;
#pragma unroll
  for (int a = 0; a < 4; ++a) vH[a] = vo + WS_H + ((mt * 4 + a) * NP + lw) * 1024;
  auto dmawd = [&](const int part) {
#pragma unroll
    for (int f = 12 * part; f < 12 * part + 12; ++f)
      if (f < 5 * WL)
        __builtin_amdgcn_raw_ptr_buffer_load_lds(rWS, (LAS void*)(wreg + wl(f)), 16, vW[f % 5], (f / 5) * 8192, 0, 16);
  };
  if (spare) {
    wait_flags(ws + WS_WF, NSP, tag, lane);
    dmawd(0);
    dmawd(1);
    dmawd(2);
  } else {
    asm volatile("s_waitcnt vmcnt(12)" ::: "memory");
    __builtin_amdgcn_s_barrier();
    issue(2, 2);
    int buf = 0;
    for (int s = 0; s < 6; ++s) {
      asm volatile("s_waitcnt vmcnt(12)" ::: "memory");
      __builtin_amdgcn_s_barrier();
      issue(s + 3, buf);
      buf = (buf == K1_NS - 1) ? 0 : buf + 1;
    }
    asm volatile("s_waitcnt vmcnt(12)" ::: "memory");
    __builtin_amdgcn_s_barrier();
    wait_flags(ws + WS_WF, NSP, tag, lane);
    issue(9, 0);
    __builtin_amdgcn_s_barrier();
    dmawd(0);
    asm volatile("s_waitcnt vmcnt(12)" ::: "memory");
    __builtin_amdgcn_s_barrier();
    dmawd(1);
    __builtin_amdgcn_s_barrier();
    dmawd(2);
  }
  {
    f32x4 acc[4][5];
#pragma unroll
    for (int a = 0; a < 4; ++a)
#pragma unroll
      for (int b = 0; b < 5; ++b) acc[a][b] = (f32x4){0.f, 0.f, 0.f, 0.f};
    constexpr int R = HRING;
    u32x4 hf[R][4], wo[2][5], we[5];
    auto ldwo = [&](const int k, u32x4(&w)[5]) {
      const int kk = (k < 13 || np == 14) ? k : 11;
#pragma unroll
      for (int b = 0; b < 5; ++b) w[b] = __builtin_amdgcn_raw_buffer_load_b128(rWS, vW[b], kk * 4096, 16);
    };
    auto ldwe = [&](const int k, u32x4(&w)[5]) {
#pragma unroll
      for (int b = 0; b < 5; ++b) w[b] = *(const u32x4*)(wreg + wl(5 * (k >> 1) + b) + vo);
    };
    auto ldh = [&](const int k, u32x4(&h)[4]) {
#pragma unroll
      for (int a = 0; a < 4; ++a) h[a] = __builtin_amdgcn_raw_buffer_load_b128(rWS, vH[a], k * 4096, 16);
    };
    unsigned ready = 0u;
    auto ensure = [&](const int k) {
      while (!((ready >> k) & 1u)) {
        ready = __builtin_amdgcn_readfirstlane(__hip_atomic_load(rdy, __ATOMIC_RELAXED, __HIP_MEMORY_SCOPE_WORKGROUP));
        if (!((ready >> k) & 1u)) __builtin_amdgcn_s_sleep(1);
      }
      asm volatile("" ::: "memory");
    };
    ldwo(1, wo[0]);
    ldwo(3, wo[1]);
    __builtin_amdgcn_sched_barrier(0);
    asm volatile("s_waitcnt vmcnt(10)" ::: "memory");
#pragma unroll
    for (int d = 0; d < R - 1; ++d) {
      ensure(d);
      ldh(d, hf[d]);
    }
    ldwe(0, we);
#pragma unroll
    for (int k = 0; k < 14; ++k) {
      const int kn = k + R - 1;
      const bool more = kn < 13;
      if (more) ensure(kn);
      else if (kn == 13 && np == 14) { ensure(13); ldh(13, hf[13 % R]); }
      __builtin_amdgcn_sched_barrier(0);
      if (k < 13 || np == 14) {
        if (more) ldh(kn, hf[kn % R]);
        if ((k & 1) == 1 && k + 1 < 14) ldwe(k + 1, we);
#pragma unroll
        for (int a = 0; a < 4; ++a)
#pragma unroll
          for (int b = 0; b < 5; ++b)
            acc[a][b] = __builtin_amdgcn_mfma_f32_16x16x32_bf16(
                __builtin_bit_cast(bf16x8, (k & 1) ? wo[(k >> 1) & 1][b] : we[b]),
                __builtin_bit_cast(bf16x8, hf[k % R][a]), acc[a][b], 0, 0, 0);
#pragma unroll
        for (int q = 0; q < 9; ++q) {
          __builtin_amdgcn_sched_group_barrier(0x008, 2, 0);
          __builtin_amdgcn_sched_group_barrier(0x120, 1, 0);
        }
        __builtin_amdgcn_sched_group_barrier(0x008, 2, 0);
      }
      __builtin_amdgcn_sched_barrier(0);
      if ((k & 1) && k + 4 < 14) ldwo(k + 4, wo[(k >> 1) & 1]);
      __builtin_amdgcn_sched_barrier(0);
    }
#pragma unroll
    for (int a = 0; a < 4; ++a)
#pragma unroll
      for (int b = 0; b < 5; ++b) *(f32x4*)(wreg + K1_STAGE + (a >> 1) * K1_STAGE + ((a & 1) * 5 + b) * 1024 + vo) = acc[a][b];
    asm volatile("s_waitcnt lgkmcnt(0)" ::: "memory");
    __builtin_amdgcn_s_barrier();
    asm volatile("" ::: "memory");
    unsigned short* op = out + (size_t)(mt * 64 + lw * 16 + l15) * D_ + nb * 80 + 4 * g;
#pragma unroll
    for (int b = 0; b < 5; ++b) {
      const char* p = sm + K1_STAGE + (lw >> 1) * K1_STAGE + ((lw & 1) * 5 + b) * 1024 + vo;
      f32x4 s = *(const f32x4*)(p);
#pragma unroll
      for (int w = 1; w < 4; ++w) s += *(const f32x4*)(p + w * 12288);
      u32x2 v;
      v[0] = pack_bf16(s[0], s[1]);
      v[1] = pack_bf16(s[2], s[3]);
      *(u32x2*)(op + b * 16) = v;
    }
  }
}

extern "C" void kernel_launch(void* const* d_in, const int* in_sizes, int n_in, void* d_out, int out_size, void* d_ws,
                              size_t ws_size, hipStream_t stream) {
  geglu_fused<<<256, 512, 0, stream>>>((const unsigned short*)d_in[0], (const unsigned short*)d_in[1],
                                       (const unsigned short*)d_in[2], (const unsigned short*)d_in[3], (char*)d_ws,
                                       (unsigned short*)d_out);
}
```

```cpp
#include <hip/hip_runtime.h>
#include <hip/hip_bf16.h>

typedef short bf16x8 __attribute__((ext_vector_type(8)));
typedef short s16x4 __attribute__((ext_vector_type(4)));
typedef float f32x4 __attribute__((ext_vector_type(4)));
typedef float f32x2 __attribute__((ext_vector_type(2)));
typedef unsigned u32x4 __attribute__((ext_vector_type(4)));
typedef unsigned u32x2 __attribute__((ext_vector_type(2)));
typedef unsigned long long u64;
#define LAS __attribute__((address_space(3)))
#define GAS __attribute__((address_space(1)))

extern "C" __device__ u64 geglu_dispatch_id() __asm("llvm.amdgcn.dispatch.id");

constexpr int M_ = 2048, D_ = 640, F_ = 1728;
constexpr int NP = F_ / 32;
constexpr int NT = D_ / 16;
constexpr int NCG = F_ / 64;
constexpr int NSP = 40;

constexpr int WS_H = 0;
constexpr int WS_H_BYTES = (M_ / 16) * NP * 1024;
constexpr int WS_WD = WS_H + WS_H_BYTES;
constexpr int WS_WD_BYTES = NT * NP * 1024;
constexpr int WS_HF = WS_WD + WS_WD_BYTES;
constexpr int WS_WF = WS_HF + 8 * 32 * 128;
constexpr int WS_END = WS_WF + 64 * 8;

constexpr int K1_A = 256 * 128;
constexpr int K1_B = 64 * 128;
constexpr int K1_STAGE = K1_A + 2 * K1_B;
constexpr int K1_NS = 3;
constexpr int K1_KT = D_ / 64;
constexpr unsigned SPIN_MAX = 1u << 18;

__device__ __forceinline__ unsigned pack_bf16(float lo, float hi) {
  unsigned r;
  asm("v_cvt_pk_bf16_f32 %0, %1, %2" : "=v"(r) : "v"(lo), "v"(hi));
  return r;
}

__device__ __forceinline__ unsigned geglu2(f32x2 g, f32x2 u) {
  const float c1 = -2.0f * 0.7978845608028654f * 1.4426950408889634f;
  const float c2 = c1 * 0.044715f;
  f32x2 t = g * ((g * g) * c2 + c1);
  f32x2 e;
  e.x = __builtin_amdgcn_exp2f(t.x);
  e.y = __builtin_amdgcn_exp2f(t.y);
  e = e + 1.0f;
  f32x2 r;
  r.x = __builtin_amdgcn_rcpf(e.x);
  r.y = __builtin_amdgcn_rcpf(e.y);
  f32x2 o = (g * r) * u;
  return pack_bf16(o.x, o.y);
}

__device__ __forceinline__ void wait_flags(const char* base, int n, u64 tag, int lane) {
  const GAS u64* p = (const GAS u64*)(base) + (lane < n ? lane : 0);
  for (unsigned spins = 0; spins < SPIN_MAX; ++spins) {
    u64 v = __hip_atomic_load(p, __ATOMIC_RELAXED, __HIP_MEMORY_SCOPE_AGENT);
    if (__all(v == tag)) break;
    __builtin_amdgcn_s_sleep(2);
  }
  __builtin_amdgcn_fence(__ATOMIC_ACQUIRE, "wavefront");
  asm volatile("" ::: "memory");
}

#define ROTF ((cl * 10 / 7 + (rl * 10 + 2) / 4 + (xcd * 5) / 4) % 10)
#define LBAR() do { asm volatile("" ::: "memory"); __builtin_amdgcn_s_barrier(); asm volatile("" ::: "memory"); } while (0)
constexpr int HRING = 3;
constexpr int FSTR = 16;
constexpr int PSLEEP = 2;

__global__ __launch_bounds__(512, 2) void geglu_fused(const unsigned short* __restrict__ x, const unsigned short* __restrict__ wg,
                                                      const unsigned short* __restrict__ wu, const unsigned short* __restrict__ wd,
                                                      char* __restrict__ ws, unsigned short* __restrict__ out) {
  __shared__ __attribute__((aligned(1024))) char sm[K1_NS * K1_STAGE + 64];
  const int tid = threadIdx.x, lane = tid & 63;
  const int wave = __builtin_amdgcn_readfirstlane(tid >> 6);
  const int bx = blockIdx.x, xcd = bx & 7, j = bx >> 3;
  const int cq = xcd & 3;
  const int ncg = cq < 3 ? 7 : 6;
  const bool spare = j >= 4 * ncg;
  const int l15 = lane & 15, g = lane >> 4;
  const int rl = j & 3, cl = j >> 2;
  const int rt = (xcd >> 2) * 4 + rl;
  const int cg = cq * 7 + cl;
  asm volatile("" ::"s"(x), "s"(wg), "s"(wu), "s"(wd), "s"(ws), "s"(out));
  const int lw = wave & 3;
  const int m0 = rt * 256;
  auto rA = __builtin_amdgcn_make_buffer_rsrc((void*)x, 0, M_ * D_ * 2, 0x00020000);
  auto rB = __builtin_amdgcn_make_buffer_rsrc((void*)(lw < 2 ? wg : wu), 0, D_ * F_ * 2, 0x00020000);
  const int r8 = lane >> 3, c8 = lane & 7;
  const int vA = (m0 + 64 * lw + r8) * (D_ * 2) + ((c8 ^ (r8 >> 1)) * 16);
  const int vA1 = (m0 + 64 * lw + r8) * (D_ * 2) + ((c8 ^ (r8 >> 1) ^ 4) * 16);
  int vB[4];
#pragma unroll
  for (int i = 0; i < 4; ++i) {
    int s = 32 * (lw & 1) + 8 * i + r8;
    int k = (s & 32) + 8 * ((s >> 2) & 3) + 4 * ((s >> 4) & 1) + (s & 3);
    vB[i] = k * (F_ * 2) + cg * 128 + ((c8 ^ (((s >> 1) & 3) << 1)) * 16);
  }
  const int ldsA = lw * 8192, ldsB = K1_A + lw * 4096;
  const int rot = ROTF;
  auto issue = [&](int kt, int buf) {
    int kr = kt - 1 + (rot % 9);
    kr = kr >= 9 ? kr - 9 : kr;
    kr = kt == 0 ? 0 : kr + 1;
    char* base = sm + buf * K1_STAGE;
#pragma unroll
    for (int i = 0; i < 4; ++i)
      __builtin_amdgcn_raw_ptr_buffer_load_lds(rB, (LAS void*)(base + ldsB + i * 1024), 16, vB[i], kr * (64 * F_ * 2), 0, 0);
#pragma unroll
    for (int i = 0; i < 8; ++i)
      __builtin_amdgcn_raw_ptr_buffer_load_lds(rA, (LAS void*)(base + ldsA + i * 1024), 16, (i & 1) ? vA1 : vA,
                                               kr * 128 + i * (8 * D_ * 2), 0, 0);
    asm volatile("" ::: "memory");
  };
  if (wave >= 4 && !spare) {
    issue(0, 0);
    issue(1, 1);
  }
  asm volatile("" ::: "memory");
  u64 tag;
  {
    u64 a = geglu_dispatch_id() * 0x9E3779B97F4A7C15ull + (u64)(size_t)__builtin_amdgcn_dispatch_ptr();
    unsigned lo = (unsigned)(a ^ (a >> 32)) * 0x85EBCA6Bu + (unsigned)geglu_dispatch_id();
    tag = ((u64)((unsigned)(a >> 6) ^ 0x5A3C96E1u) << 32) | (u64)lo;
  }
  auto rWS = __builtin_amdgcn_make_buffer_rsrc((void*)ws, 0, WS_END, 0x00020000);
  const int mt = xcd * 4 + (j >> 3);
  const int nb = j & 7;
  const int np = lw < 2 ? 14 : 13;
  LAS unsigned* rdy = (LAS unsigned*)(sm + K1_NS * K1_STAGE) + lw * 4;
  LAS unsigned* cnt = (LAS unsigned*)(sm + K1_NS * K1_STAGE) + 15;
  if (wave < 4 && lane == 0) *rdy = 0u;
  if (tid == 0) *cnt = 0u;
  if (spare) {
    const int sp = (cq < 3 ? (xcd >> 2) * 20 + cq * 4 + (j - 28) : (xcd >> 2) * 20 + 12 + (j - 24));
    const int gw = sp * 8 + wave;
    if (gw < NCG * 10) {
      const int fb = gw / 10, nbk = gw - fb * 10;
      auto rD = __builtin_amdgcn_make_buffer_rsrc((void*)wd, 0, F_ * D_ * 2, 0x00020000);
      const int r8 = lane >> 3, c8 = lane & 7;
      char* my = sm + wave * 8192;
      const int vD = (fb * 64 + r8) * (D_ * 2) + nbk * 128 + ((c8 ^ (((r8 >> 1) & 3) << 1)) * 16);
#pragma unroll
      for (int i = 0; i < 8; ++i)
        __builtin_amdgcn_raw_ptr_buffer_load_lds(rD, (LAS void*)(my + i * 1024), 16, vD, i * (8 * D_ * 2), 0, 0);
      asm volatile("s_waitcnt vmcnt(0)" ::: "memory");
      const int q = l15 >> 2, lx = (2 * g + (q >> 1)) & 3;
#pragma unroll
      for (int pp = 0; pp < 2; ++pp)
#pragma unroll
        for (int u = 0; u < 4; ++u) {
          const char* p = my + (32 * pp + 4 * g + q) * 128 + (lane & 3) * 8 + ((u ^ lx) * 32);
          s16x4 lo = __builtin_amdgcn_ds_read_tr16_b64_v4i16((LAS s16x4*)(p));
          s16x4 hi = __builtin_amdgcn_ds_read_tr16_b64_v4i16((LAS s16x4*)(p + 2048));
          bf16x8 v = __builtin_shufflevector(lo, hi, 0, 1, 2, 3, 4, 5, 6, 7);
          __builtin_amdgcn_raw_buffer_store_b128(__builtin_bit_cast(u32x4, v), rWS,
                                                 WS_WD + (((nbk * 4 + u) * NP + fb * 2 + pp) * 64 + lane) * 16, 0, 16);
        }
    }
    asm volatile("s_waitcnt vmcnt(0) lgkmcnt(0)" ::: "memory");
    __builtin_amdgcn_s_barrier();
    asm volatile("" ::: "memory");
    if (tid == 256) __hip_atomic_store((GAS u64*)(ws + WS_WF) + sp, tag, __ATOMIC_RELAXED, __HIP_MEMORY_SCOPE_AGENT);
  }
  if (wave < 4) {
    if (!spare) {
      const int mh = wave >> 1, fh = wave & 1;
      f32x4 acc[2][2][8];
#pragma unroll
      for (int a = 0; a < 2; ++a)
#pragma unroll
        for (int b = 0; b < 2; ++b)
#pragma unroll
          for (int c = 0; c < 8; ++c) acc[a][b][c] = (f32x4){0.f, 0.f, 0.f, 0.f};
      const int swz = l15 >> 1;
      const int aoff0 = (128 * mh + l15) * 128 + ((g ^ swz) * 16);
      const int aoff1 = (128 * mh + l15) * 128 + (((4 + g) ^ swz) * 16);
      const int q = l15 >> 2, lx = (2 * g + (q >> 1)) & 3;
      const int bo0 = K1_A + (4 * g + q) * 128 + (lane & 3) * 8 + (((2 * fh) ^ lx) * 32);
      const int bo1 = K1_A + (4 * g + q) * 128 + (lane & 3) * 8 + (((2 * fh + 1) ^ lx) * 32);

      bf16x8 xa[8], wa[2][2], xb[8], wb[2][2];
      auto rd = [&](const char* base, int kk, bf16x8(&xf)[8], bf16x8(&wf)[2][2]) {
#pragma unroll
        for (int u = 0; u < 2; ++u)
#pragma unroll
          for (int mat = 0; mat < 2; ++mat) {
            const char* p = base + (u ? bo1 : bo0) + mat * K1_B + kk * 4096;
            s16x4 lo = __builtin_amdgcn_ds_read_tr16_b64_v4i16((LAS s16x4*)(p));
            s16x4 hi = __builtin_amdgcn_ds_read_tr16_b64_v4i16((LAS s16x4*)(p + 2048));
            wf[u][mat] = __builtin_shufflevector(lo, hi, 0, 1, 2, 3, 4, 5, 6, 7);
          }
#pragma unroll
        for (int rb = 0; rb < 8; ++rb) xf[rb] = *(const bf16x8*)(base + (kk ? aoff1 : aoff0) + rb * 2048);
      };
      auto mm = [&](bf16x8(&xf)[8], bf16x8(&wf)[2][2]) {
#pragma unroll
        for (int rb = 0; rb < 8; ++rb)
#pragma unroll
          for (int u = 0; u < 2; ++u)
#pragma unroll
            for (int mat = 0; mat < 2; ++mat)
              acc[u][mat][rb] = __builtin_amdgcn_mfma_f32_16x16x32_bf16(wf[u][mat], xf[rb], acc[u][mat][rb], 0, 0, 0);
      };
      auto sched = [&]() {
#pragma unroll
        for (int i = 0; i < 16; ++i) {
          __builtin_amdgcn_sched_group_barrier(0x100, 1, 0);
          __builtin_amdgcn_sched_group_barrier(0x008, 1, 0);
        }
        __builtin_amdgcn_sched_group_barrier(0x008, 16, 0);
      };
      asm volatile("s_waitcnt lgkmcnt(0)" ::: "memory");
      __builtin_amdgcn_s_barrier();
      asm volatile("" ::: "memory");
      rd(sm, 0, xa, wa);
      __builtin_amdgcn_sched_barrier(0);
      __builtin_amdgcn_s_waitcnt(0xC07F);
      __builtin_amdgcn_sched_barrier(0);
      int buf = 0;
      for (int s = 0; s < K1_KT; ++s) {
        const char* cur = sm + buf * K1_STAGE;
        buf = (buf == K1_NS - 1) ? 0 : buf + 1;
        const char* nxt = sm + buf * K1_STAGE;
        rd(cur, 1, xb, wb);
        mm(xa, wa);
        sched();
        __builtin_amdgcn_sched_barrier(0);
        __builtin_amdgcn_s_waitcnt(0xC07F);
        asm volatile("" ::: "memory");
        __builtin_amdgcn_s_barrier();
        asm volatile("" ::: "memory");
        __builtin_amdgcn_sched_barrier(0);
        rd(nxt, 0, xa, wa);
        mm(xb, wb);
        sched();
        __builtin_amdgcn_sched_barrier(0);
        __builtin_amdgcn_s_waitcnt(0xC07F);
        __builtin_amdgcn_sched_barrier(0);
      }
      const int hoff = WS_H + (((rt * 16 + mh * 8) * NP + cg * 2 + fh) * 64 + lane) * 16;
#pragma unroll
      for (int rb = 0; rb < 8; ++rb) {
        u32x4 v;
#pragma unroll
        for (int u = 0; u < 2; ++u) {
          const f32x4 gg = acc[u][0][rb], uu = acc[u][1][rb];
          v[2 * u] = geglu2((f32x2){gg[0], gg[1]}, (f32x2){uu[0], uu[1]});
          v[2 * u + 1] = geglu2((f32x2){gg[2], gg[3]}, (f32x2){uu[2], uu[3]});
        }
        __builtin_amdgcn_raw_buffer_store_b128(v, rWS, hoff + rb * NP * 1024, 0, 16);
      }
      asm volatile("s_waitcnt vmcnt(0)" ::: "memory");
      if (lane == 0) {
        const unsigned old = __hip_atomic_fetch_add(cnt, 1u, __ATOMIC_RELAXED, __HIP_MEMORY_SCOPE_WORKGROUP);
        if (old == 3u) __hip_atomic_store((GAS u64*)(ws + WS_HF) + rt * FSTR * 32 + cg * FSTR, tag, __ATOMIC_RELAXED, __HIP_MEMORY_SCOPE_AGENT);
      }
    }
    {
      const GAS u64* fp = (const GAS u64*)(ws + WS_HF) + (mt >> 2) * FSTR * 32 + ((4 * (lane < np ? lane : 0) + lw) >> 1) * FSTR;
      const unsigned all = (1u << np) - 1u;
      unsigned seen = 0u;
      for (unsigned spins = 0; seen != all; ++spins) {
        u64 v = __hip_atomic_load(fp, __ATOMIC_RELAXED, __HIP_MEMORY_SCOPE_AGENT);
        unsigned m = (unsigned)__ballot(v == tag) & all;
        if (spins >= SPIN_MAX) m = all;
        if (m != seen) {
          seen = m;
          if (lane == 0) __hip_atomic_store(rdy, m, __ATOMIC_RELAXED, __HIP_MEMORY_SCOPE_WORKGROUP);
        } else {
          __builtin_amdgcn_s_sleep(PSLEEP);
        }
      }
    }
    return;
  }
  const int vo = lane * 16;
  constexpr int WL = 7;
  char* wreg = sm + lw * 12288;
  auto wl = [](const int f) { return ((f / 12 + 1) % 3) * K1_STAGE + (f % 12) * 1024; };
  int vW[5], vH[4];
#pragma unroll
  for (int b = 0; b < 5; ++b) vW[b] = vo + WS_WD + ((nb * 5 + b) * NP + lw) * 1024;
#pragma unroll
  for (int a = 0; a < 4; ++a) vH[a] = vo + WS_H + ((mt * 4 + a) * NP + lw) * 1024;
  auto dmawd = [&](const int part) {
#pragma unroll
    for (int f = 12 * part; f < 12 * part + 12; ++f)
      if (f < 5 * WL)
        __builtin_amdgcn_raw_ptr_buffer_load_lds(rWS, (LAS void*)(wreg + wl(f)), 16, vW[f % 5], (f / 5) * 8192, 0, 16);
    asm volatile("" ::: "memory");
  };
  if (spare) {
    wait_flags(ws + WS_WF, NSP, tag, lane);
    dmawd(0);
    dmawd(1);
    dmawd(2);
  } else {
    asm volatile("s_waitcnt vmcnt(12)" ::: "memory");
    LBAR();
    issue(2, 2);
    int buf = 0;
    for (int s = 0; s < 6; ++s) {
      asm volatile("s_waitcnt vmcnt(12)" ::: "memory");
      LBAR();
      issue(s + 3, buf);
      buf = (buf == K1_NS - 1) ? 0 : buf + 1;
    }
    asm volatile("s_waitcnt vmcnt(12)" ::: "memory");
    LBAR();
    wait_flags(ws + WS_WF, NSP, tag, lane);
    asm volatile("s_waitcnt vmcnt(0)" ::: "memory");
    issue(9, 0);
    LBAR();
    dmawd(0);
    asm volatile("s_waitcnt vmcnt(12)" ::: "memory");
    LBAR();
    dmawd(1);
    LBAR();
    dmawd(2);
  }
  {
    f32x4 acc[4][5];
#pragma unroll
    for (int a = 0; a < 4; ++a)
#pragma unroll
      for (int b = 0; b < 5; ++b) acc[a][b] = (f32x4){0.f, 0.f, 0.f, 0.f};
    constexpr int R = HRING;
    u32x4 hf[R][4], wo[2][5], we[5];
    auto ldwo = [&](const int k, u32x4(&w)[5]) {
      const int kk = (k < 13 || np == 14) ? k : 11;
#pragma unroll
      for (int b = 0; b < 5; ++b) w[b] = __builtin_amdgcn_raw_buffer_load_b128(rWS, vW[b], kk * 4096, 16);
    };
    auto ldwe = [&](const int k, u32x4(&w)[5]) {
#pragma unroll
      for (int b = 0; b < 5; ++b) w[b] = *(const u32x4*)(wreg + wl(5 * (k >> 1) + b) + vo);
    };
    auto ldh = [&](const int k, u32x4(&h)[4]) {
#pragma unroll
      for (int a = 0; a < 4; ++a) h[a] = __builtin_amdgcn_raw_buffer_load_b128(rWS, vH[a], k * 4096, 16);
    };
    unsigned ready = 0u;
    auto ensure = [&](const int k) {
      while (!((ready >> k) & 1u)) {
        ready = __builtin_amdgcn_readfirstlane(__hip_atomic_load(rdy, __ATOMIC_RELAXED, __HIP_MEMORY_SCOPE_WORKGROUP));
        if (!((ready >> k) & 1u)) __builtin_amdgcn_s_sleep(1);
      }
      asm volatile("" ::: "memory");
    };
    ldwo(1, wo[0]);
    ldwo(3, wo[1]);
    __builtin_amdgcn_sched_barrier(0);
    asm volatile("s_waitcnt vmcnt(10)" ::: "memory");
#pragma unroll
    for (int d = 0; d < R - 1; ++d) {
      ensure(d);
      ldh(d, hf[d]);
    }
    ldwe(0, we);
#pragma unroll
    for (int k = 0; k < 14; ++k) {
      const int kn = k + R - 1;
      const bool more = kn < 13;
      if (more) ensure(kn);
      else if (kn == 13 && np == 14) { ensure(13); ldh(13, hf[13 % R]); }
      __builtin_amdgcn_sched_barrier(0);
      if (k < 13 || np == 14) {
        if (more) ldh(kn, hf[kn % R]);
        if ((k & 1) == 1 && k + 1 < 14) ldwe(k + 1, we);
#pragma unroll
        for (int a = 0; a < 4; ++a)
#pragma unroll
          for (int b = 0; b < 5; ++b)
            acc[a][b] = __builtin_amdgcn_mfma_f32_16x16x32_bf16(
                __builtin_bit_cast(bf16x8, (k & 1) ? wo[(k >> 1) & 1][b] : we[b]),
                __builtin_bit_cast(bf16x8, hf[k % R][a]), acc[a][b], 0, 0, 0);
#pragma unroll
        for (int q = 0; q < 9; ++q) {
          __builtin_amdgcn_sched_group_barrier(0x008, 2, 0);
          __builtin_amdgcn_sched_group_barrier(0x120, 1, 0);
        }
        __builtin_amdgcn_sched_group_barrier(0x008, 2, 0);
      }
      __builtin_amdgcn_sched_barrier(0);
      if ((k & 1) && k + 4 < 14) ldwo(k + 4, wo[(k >> 1) & 1]);
      __builtin_amdgcn_sched_barrier(0);
    }
#pragma unroll
    for (int a = 0; a < 4; ++a)
#pragma unroll
      for (int b = 0; b < 5; ++b) *(f32x4*)(wreg + K1_STAGE + (a >> 1) * K1_STAGE + ((a & 1) * 5 + b) * 1024 + vo) = acc[a][b];
    asm volatile("s_waitcnt lgkmcnt(0)" ::: "memory");
    __builtin_amdgcn_s_barrier();
    asm volatile("" ::: "memory");
    unsigned short* op = out + (size_t)(mt * 64 + lw * 16 + l15) * D_ + nb * 80 + 4 * g;
#pragma unroll
    for (int b = 0; b < 5; ++b) {
      const char* p = sm + K1_STAGE + (lw >> 1) * K1_STAGE + ((lw & 1) * 5 + b) * 1024 + vo;
      f32x4 s = *(const f32x4*)(p);
#pragma unroll
      for (int w = 1; w < 4; ++w) s += *(const f32x4*)(p + w * 12288);
      u32x2 v;
      v[0] = pack_bf16(s[0], s[1]);
      v[1] = pack_bf16(s[2], s[3]);
      *(u32x2*)(op + b * 16) = v;
    }
  }
}

extern "C" void kernel_launch(void* const* d_in, const int* in_sizes, int n_in, void* d_out, int out_size, void* d_ws,
                              size_t ws_size, hipStream_t stream) {
  geglu_fused<<<256, 512, 0, stream>>>((const unsigned short*)d_in[0], (const unsigned short*)d_in[1],
                                       (const unsigned short*)d_in[2], (const unsigned short*)d_in[3], (char*)d_ws,
                                       (unsigned short*)d_out);
}
```
